```python
import torch

def _softmax(x, axis=-1):
    return torch.softmax(x, dim=axis)
def _gelu(x):
    return torch.nn.functional.gelu(x, approximate="tanh")
def _silu(x):
    return torch.nn.functional.silu(x)
def _sigmoid(x):
    return torch.sigmoid(x)
def _layernorm(x, axis=-1, eps=1e-5):
    m = x.mean(dim=axis, keepdim=True)
    v = x.var(dim=axis, keepdim=True, unbiased=False)
    return (x - m) / torch.sqrt(v + eps)
def _scatter_add(tgt, idx, src, axis=0):
    return tgt.clone().index_add_(axis, idx, src)
def _segment_sum(src, seg, n):
    out = torch.zeros((n,) + src.shape[1:], dtype=src.dtype)
    return out.index_add_(0, seg, src)
def _pad(x, pw):
    flat = tuple(v for pair in reversed(pw) for v in pair)
    return torch.nn.functional.pad(x, flat)

kernel_name = 'parallel_sum__ssm_conv_s4d__glu_gated_reglu__prompt'

def setup_inputs():

    _w = (lambda a: (lambda t: t.to(torch.bfloat16) if t.is_floating_point() else t)(torch.as_tensor(a)))
    def _A_setup():
        Bsz, L, D, N = 1, 2048, 448, 256
        return {
            "u": _w((lambda *s: torch.randn(*s, dtype=torch.bfloat16) * 0.05)(Bsz, L, D)),
            "log_A_real": _w((lambda *s: torch.randn(*s, dtype=torch.bfloat16) * 6.4)(D, N) * 0.5),
            "A_imag": _w((lambda *s: torch.randn(*s, dtype=torch.bfloat16) * 0.05)(D, N) * 3.14),
            "C_real": _w((lambda *s: torch.randn(*s, dtype=torch.bfloat16) * 0.1)(D, N) / N ** 0.5),
            "C_imag": _w((lambda *s: torch.randn(*s, dtype=torch.bfloat16) * 0.1)(D, N) / N ** 0.5),
            "log_dt": _w((lambda *s: torch.randn(*s, dtype=torch.bfloat16) * 0.05)(D) * 0.5 - 4.0),
            "D_skip": _w((lambda *s: torch.randn(*s, dtype=torch.bfloat16) * 0.05)(D)),
        }
    _A_in = _A_setup()

    def _B_setup():
        B, S, D, F = 1, 2048, 448, 1792
        return {
            "x": _w((lambda *s: torch.randn(*s, dtype=torch.bfloat16) * 0.05)(B, S, D)),
            "W_gate": _w((lambda *s: torch.randn(*s, dtype=torch.bfloat16) * 0.4)(D, F) * (2.0 / D) ** 0.5),
            "W_up": _w((lambda *s: torch.randn(*s, dtype=torch.bfloat16) * 0.4)(D, F) * (2.0 / D) ** 0.5),
            "W_down": _w((lambda *s: torch.randn(*s, dtype=torch.bfloat16) * 0.4)(F, D) * (1.0 / F) ** 0.5),
        }
    _B_in = _B_setup()

    return {
        "A_u": _A_in["u"],
        "A_log_A_real": _A_in["log_A_real"],
        "A_A_imag": _A_in["A_imag"],
        "A_C_real": _A_in["C_real"],
        "A_C_imag": _A_in["C_imag"],
        "A_log_dt": _A_in["log_dt"],
        "A_D_skip": _A_in["D_skip"],
        "B_x": _B_in["x"],
        "B_W_gate": _B_in["W_gate"],
        "B_W_up": _B_in["W_up"],
        "B_W_down": _B_in["W_down"],
    }

def reference(A_u, A_log_A_real, A_A_imag, A_C_real, A_C_imag, A_log_dt, A_D_skip, B_x, B_W_gate, B_W_up, B_W_down):
    def _A_ref(u, log_A_real, A_imag, C_real, C_imag, log_dt, D_skip):
        L = u.shape[1]
        dt = torch.exp(log_dt)
        A = -torch.exp(log_A_real) + 1j * A_imag
        C = (C_real + 1j * C_imag) * dt[:, None]
        pos = (lambda x, dt: x.to(dt))(torch.arange(L), log_dt.dtype)
        P = torch.exp((A * dt[:, None])[:, :, None] * pos[None, None, :])
        K = 2.0 * torch.einsum("dn,dnl->dl", C, P).real
        L2 = 2 * L
        u_t = (lambda x, *d: x.permute(*d))(u, 0, 2, 1)
        Kf = (lambda x, *a, **k: torch.fft.rfft(x.float(), *a, **k))(K, L2)
        Uf = (lambda x, *a, **k: torch.fft.rfft(x.float(), *a, **k))(u_t, L2)
        y = (lambda x, *a, **k: torch.fft.irfft(x, *a, **k).to(torch.bfloat16))(Uf * Kf[None, :, :], L2)[:, :, :L]
        y = (lambda x, *d: x.permute(*d))(y, 0, 2, 1)
        return y + u * D_skip[None, None, :]

    def _B_ref(x, W_gate, W_up, W_down):
        gate = torch.einsum("bsd,df->bsf", x, W_gate)
        up = torch.einsum("bsd,df->bsf", x, W_up)
        gate = gate * (lambda x, dt: x.to(dt))(gate > 0, gate.dtype)
        h = gate * up
        return torch.einsum("bsf,fd->bsd", h, W_down)

    x = A_u
    xn = _layernorm(x, axis=-1)
    return x + _A_ref(xn, A_log_A_real, A_A_imag, A_C_real, A_C_imag, A_log_dt, A_D_skip) + _B_ref(xn, B_W_gate, B_W_up, B_W_down)
```

```cpp
#include <hip/hip_runtime.h>
#include <hip/hip_bf16.h>
#include <stdint.h>
typedef unsigned short u16;
constexpr int L=2048, D=448, N=256, F=1792;
__device__ __forceinline__ float bf2f(u16 v){ return __uint_as_float(((unsigned)v)<<16); }
__device__ __forceinline__ u16 f2bf(float f){ unsigned u=__float_as_uint(f); return (u16)((u+0x7FFFu+((u>>16)&1))>>16); }
__device__ __forceinline__ float rbf(float f){ return bf2f(f2bf(f)); }

__global__ void k_ln(const u16* x, u16* xn){
  int row=blockIdx.x; int t=threadIdx.x;
  float v[7]; float s=0;
  for(int i=0;i<7;i++){ v[i]=bf2f(x[row*D+t+64*i]); s+=v[i]; }
  for(int o=32;o;o>>=1) s+=__shfl_xor(s,o);
  float m=s/D; float q=0;
  for(int i=0;i<7;i++){ float d=v[i]-m; q+=d*d; }
  for(int o=32;o;o>>=1) q+=__shfl_xor(q,o);
  float mb=rbf(m); float vb=rbf(q/D); float sd=rbf(sqrtf(rbf(vb+1e-5f)));
  for(int i=0;i<7;i++) xn[row*D+t+64*i]=f2bf(rbf(v[i]-mb)/sd);
}
__global__ void k_gen(const u16* lar,const u16* aim,const u16* cr,const u16* ci,const u16* ldt,float* K){
  int d=blockIdx.x;
  __shared__ float a[N],b[N],c0[N],c1[N];
  float dt=rbf(expf(bf2f(ldt[d])));
  int n=threadIdx.x;
  a[n]=-rbf(expf(bf2f(lar[d*N+n])))*dt; b[n]=bf2f(aim[d*N+n])*dt; c0[n]=bf2f(cr[d*N+n])*dt; c1[n]=bf2f(ci[d*N+n])*dt;
  __syncthreads();
  for(int l=threadIdx.x;l<L;l+=256){
    float p=rbf((float)l); float s=0;
    for(int k=0;k<N;k++){ float e=__expf(a[k]*p); float sn,cs; __sincosf(b[k]*p,&sn,&cs); s+=e*(c0[k]*cs-c1[k]*sn); }
    K[d*L+l]=2.f*s;
  }
}
__global__ void k_conv(const u16* xn,const float* K,float* y){
  int d=blockIdx.x; __shared__ float k[L],u[L];
  for(int l=threadIdx.x;l<L;l+=256){ k[l]=K[d*L+l]; u[l]=bf2f(xn[l*D+d]); }
  __syncthreads();
  for(int t=threadIdx.x;t<L;t+=256){ float s=0; for(int j=0;j<=t;j++) s+=k[j]*u[t-j]; y[t*D+d]=s; }
}
__global__ void k_g1(const u16* xn,const u16* wg,const u16* wu,u16* h){
  int r0=blockIdx.y*16; int f=blockIdx.x*256+threadIdx.x;
  __shared__ float xs[16][D];
  for(int i=threadIdx.x;i<16*D;i+=256) xs[i/D][i%D]=bf2f(xn[r0*D+i]);
  __syncthreads();
  float g[16],u[16]; for(int i=0;i<16;i++){g[i]=0;u[i]=0;}
  for(int k=0;k<D;k++){ float a=bf2f(wg[k*F+f]),b=bf2f(wu[k*F+f]);
#pragma unroll
    for(int i=0;i<16;i++){ g[i]+=xs[i][k]*a; u[i]+=xs[i][k]*b; } }
  for(int i=0;i<16;i++){ float gg=rbf(g[i]); gg=gg>0?gg:0.f; h[(r0+i)*F+f]=f2bf(gg*rbf(u[i])); }
}
__global__ void k_g2(const u16* x,const u16* xn,const u16* h,const u16* wd,const float* y,const u16* dsk,u16* out){
  int r0=blockIdx.x*8; int d=threadIdx.x;
  __shared__ float hs[8][F];
  for(int i=threadIdx.x;i<8*F;i+=448) hs[i/F][i%F]=bf2f(h[r0*F+i]);
  __syncthreads();
  float a[8]; for(int i=0;i<8;i++)a[i]=0;
  for(int k=0;k<F;k++){ float w=bf2f(wd[k*D+d]);
#pragma unroll
    for(int i=0;i<8;i++) a[i]+=hs[i][k]*w; }
  float ds=bf2f(dsk[d]);
  for(int i=0;i<8;i++){ int o=(r0+i)*D+d; float A=rbf(rbf(y[o])+rbf(bf2f(xn[o])*ds)); out[o]=f2bf(rbf(bf2f(x[o])+A)+rbf(a[i])); }
}
extern "C" void kernel_launch(void* const* d_in, const int* in_sizes, int n_in, void* d_out, int out_size, void* d_ws, size_t ws_size, hipStream_t stream){
  const u16* x=(const u16*)d_in[0];
  char* ws=(char*)d_ws;
  u16* xn=(u16*)ws; float* K=(float*)(ws+(4<<20)); float* y=(float*)(ws+(8<<20)); u16* h=(u16*)(ws+(16<<20));
  k_ln<<<L,64,0,stream>>>(x,xn);
  k_gen<<<D,256,0,stream>>>((const u16*)d_in[1],(const u16*)d_in[2],(const u16*)d_in[3],(const u16*)d_in[4],(const u16*)d_in[5],K);
  k_conv<<<D,256,0,stream>>>(xn,K,y);
  k_g1<<<dim3(F/256,L/16),256,0,stream>>>(xn,(const u16*)d_in[8],(const u16*)d_in[9],h);
  k_g2<<<L/8,448,0,stream>>>(x,xn,h,(const u16*)d_in[10],y,(const u16*)d_in[6],(u16*)d_out);
}
```

```cpp
#include <hip/hip_runtime.h>
#include <hip/hip_bf16.h>
#include <stdint.h>
constexpr unsigned XB=2u;
constexpr unsigned AGW=7u;
constexpr unsigned ESLP=24u;
constexpr unsigned TS=114816u;
constexpr unsigned HOFF=224u;
constexpr unsigned US=4224u;
constexpr unsigned WS_E=0u;
constexpr unsigned WS_FHLP=11546624u;
constexpr unsigned WS_FH=11677696u;
constexpr unsigned WS_FY=11808768u;
constexpr unsigned WS_STM=65536u;
constexpr unsigned WS_STR=25600u;
constexpr unsigned WS_CS=33792u;
constexpr unsigned WS_UT=131072u;
constexpr unsigned WS_Y=2097152u;
constexpr unsigned WS_H=4194304u;
constexpr unsigned WS_FW=65536u;
constexpr unsigned WS_DBG=11939840u;
constexpr unsigned WS_SIZE=12464128u;
#define HPF 0
#define CSF 0
#define CPF 1
constexpr int SPIN_LIMIT=1<<20;
typedef unsigned short u16;
typedef unsigned long long u64;
typedef __attribute__((ext_vector_type(4))) unsigned u32x4;
typedef __attribute__((ext_vector_type(2))) unsigned u32x2;
typedef __attribute__((ext_vector_type(4))) short s16x4;
typedef __attribute__((address_space(3))) s16x4 lds_s16x4;
typedef __attribute__((address_space(1))) u64 gu64;
typedef __attribute__((address_space(1))) u32x4 g32x4;
__device__ const u16 GT[2560] __attribute__((aligned(16))) = {2560,2560,2560,2560,2556,2556,2556,2556,2552,2552,2552,2552,2548,2548,2548,2548,2544,2544,2544,2544,2540,2540,2540,2540,2536,2536,2536,2536,2532,2532,2532,2532,2528,2528,2528,2528,2524,2524,2524,2524,2520,2520,2520,2520,2516,2516,2516,2516,2512,2512,2512,2512,2508,2508,2508,2508,2504,2504,2504,2504,2500,2500,2500,2500,2496,2496,2496,2496,2492,2492,2492,2492,2488,2488,2488,2488,2484,2484,2484,2484,2480,2480,2480,2480,2476,2476,2476,2476,2472,2472,2472,2472,2468,2468,2468,2468,2464,2464,2464,2464,2460,2460,2460,2460,2456,2456,2456,2456,2452,2452,2452,2452,2448,2448,2448,2448,2444,2444,2444,2444,2440,2440,2440,2440,2436,2436,2436,2436,2432,2432,2432,2432,2428,2428,2428,2428,2424,2424,2424,2424,2420,2420,2420,2420,2416,2416,2416,2416,2412,2412,2412,2412,2408,2408,2408,2408,2404,2404,2404,2404,2400,2400,2400,2400,2396,2396,2396,2396,2392,2392,2392,2392,2388,2388,2388,2388,2384,2384,2384,2384,2380,2380,2380,2380,2376,2376,2376,2376,2372,2372,2372,2372,2368,2368,2368,2368,2364,2364,2364,2364,2360,2360,2360,2360,2356,2356,2356,2356,2352,2352,2352,2352,2348,2348,2348,2348,2344,2344,2344,2344,2340,2340,2340,2340,2336,2336,2336,2336,2332,2332,2332,2332,2328,2328,2328,2328,2324,2324,2324,2324,2320,2320,2320,2320,2316,2316,2316,2316,2312,2312,2312,2312,2308,2308,2308,2308,2304,2304,2304,2304,2300,2300,2300,2300,2296,2296,2296,2296,2292,2292,2292,2292,2288,2288,2288,2288,2284,2284,2284,2284,2280,2280,2280,2280,2276,2276,2276,2276,2272,2272,2272,2272,2268,2268,2268,2268,2264,2264,2264,2264,2260,2260,2260,2260,2256,2256,2256,2256,2252,2252,2252,2252,2248,2248,2248,2248,2244,2244,2244,2244,2240,2240,2240,2240,2236,2236,2236,2236,2232,2232,2232,2232,2228,2228,2228,2228,2224,2224,2224,2224,2220,2220,2220,2220,2216,2216,2216,2216,2212,2212,2212,2212,2208,2208,2208,2208,2204,2204,2204,2204,2200,2200,2200,2200,2196,2196,2196,2196,2192,2192,2192,2192,2188,2188,2188,2188,2184,2184,2184,2184,2180,2180,2180,2180,2176,2176,2176,2176,2172,2172,2172,2172,2168,2168,2168,2168,2164,2164,2164,2164,2160,2160,2160,2160,2156,2156,2156,2156,2152,2152,2152,2152,2148,2148,2148,2148,2144,2144,2144,2144,2140,2140,2140,2140,2136,2136,2136,2136,2132,2132,2132,2132,2128,2128,2128,2128,2124,2124,2124,2124,2120,2120,2120,2120,2116,2116,2116,2116,2112,2112,2112,2112,2108,2108,2108,2108,2104,2104,2104,2104,2100,2100,2100,2100,2096,2096,2096,2096,2092,2092,2092,2092,2088,2088,2088,2088,2084,2084,2084,2084,2080,2080,2080,2080,2076,2076,2076,2076,2072,2072,2072,2072,2068,2068,2068,2068,2064,2064,2064,2064,2060,2060,2060,2060,2056,2056,2056,2056,2052,2052,2052,2052,2556,2556,2556,2552,2552,2552,2552,2552,2548,2548,2548,2544,2544,2544,2544,2544,2540,2540,2540,2536,2536,2536,2536,2536,2532,2532,2532,2528,2528,2528,2528,2528,2524,2524,2524,2520,2520,2520,2520,2520,2516,2516,2516,2512,2512,2512,2512,2512,2508,2508,2508,2504,2504,2504,2504,2504,2500,2500,2500,2496,2496,2496,2496,2496,2492,2492,2492,2488,2488,2488,2488,2488,2484,2484,2484,2480,2480,2480,2480,2480,2476,2476,2476,2472,2472,2472,2472,2472,2468,2468,2468,2464,2464,2464,2464,2464,2460,2460,2460,2456,2456,2456,2456,2456,2452,2452,2452,2448,2448,2448,2448,2448,2444,2444,2444,2440,2440,2440,2440,2440,2436,2436,2436,2432,2432,2432,2432,2432,2428,2428,2428,2424,2424,2424,2424,2424,2420,2420,2420,2416,2416,2416,2416,2416,2412,2412,2412,2408,2408,2408,2408,2408,2404,2404,2404,2400,2400,2400,2400,2400,2396,2396,2396,2392,2392,2392,2392,2392,2388,2388,2388,2384,2384,2384,2384,2384,2380,2380,2380,2376,2376,2376,2376,2376,2372,2372,2372,2368,2368,2368,2368,2368,2364,2364,2364,2360,2360,2360,2360,2360,2356,2356,2356,2352,2352,2352,2352,2352,2348,2348,2348,2344,2344,2344,2344,2344,2340,2340,2340,2336,2336,2336,2336,2336,2332,2332,2332,2328,2328,2328,2328,2328,2324,2324,2324,2320,2320,2320,2320,2320,2316,2316,2316,2312,2312,2312,2312,2312,2308,2308,2308,2304,2304,2304,2304,2304,2300,2300,2300,2296,2296,2296,2296,2296,2292,2292,2292,2288,2288,2288,2288,2288,2284,2284,2284,2280,2280,2280,2280,2280,2276,2276,2276,2272,2272,2272,2272,2272,2268,2268,2268,2264,2264,2264,2264,2264,2260,2260,2260,2256,2256,2256,2256,2256,2252,2252,2252,2248,2248,2248,2248,2248,2244,2244,2244,2240,2240,2240,2240,2240,2236,2236,2236,2232,2232,2232,2232,2232,2228,2228,2228,2224,2224,2224,2224,2224,2220,2220,2220,2216,2216,2216,2216,2216,2212,2212,2212,2208,2208,2208,2208,2208,2204,2204,2204,2200,2200,2200,2200,2200,2196,2196,2196,2192,2192,2192,2192,2192,2188,2188,2188,2184,2184,2184,2184,2184,2180,2180,2180,2176,2176,2176,2176,2176,2172,2172,2172,2168,2168,2168,2168,2168,2164,2164,2164,2160,2160,2160,2160,2160,2156,2156,2156,2152,2152,2152,2152,2152,2148,2148,2148,2144,2144,2144,2144,2144,2140,2140,2140,2136,2136,2136,2136,2136,2132,2132,2132,2128,2128,2128,2128,2128,2124,2124,2124,2120,2120,2120,2120,2120,2116,2116,2116,2112,2112,2112,2112,2112,2108,2108,2108,2104,2104,2104,2104,2104,2100,2100,2100,2096,2096,2096,2096,2096,2092,2092,2092,2088,2088,2088,2088,2088,2084,2084,2084,2080,2080,2080,2080,2080,2076,2076,2076,2072,2072,2072,2072,2072,2068,2068,2068,2064,2064,2064,2064,2064,2060,2060,2060,2056,2056,2056,2056,2056,2052,2052,2052,2048,2048,2048,2048,2048,2552,2552,2552,2552,2544,2544,2544,2544,2536,2536,2536,2536,2528,2528,2528,2528,2520,2520,2520,2520,2512,2512,2512,2512,2504,2504,2504,2504,2496,2496,2496,2496,2488,2488,2488,2488,2480,2480,2480,2480,2472,2472,2472,2472,2464,2464,2464,2464,2456,2456,2456,2456,2448,2448,2448,2448,2440,2440,2440,2440,2432,2432,2432,2432,2424,2424,2424,2424,2416,2416,2416,2416,2408,2408,2408,2408,2400,2400,2400,2400,2392,2392,2392,2392,2384,2384,2384,2384,2376,2376,2376,2376,2368,2368,2368,2368,2360,2360,2360,2360,2352,2352,2352,2352,2344,2344,2344,2344,2336,2336,2336,2336,2328,2328,2328,2328,2320,2320,2320,2320,2312,2312,2312,2312,2304,2304,2304,2304,2296,2296,2296,2296,2288,2288,2288,2288,2280,2280,2280,2280,2272,2272,2272,2272,2264,2264,2264,2264,2256,2256,2256,2256,2248,2248,2248,2248,2240,2240,2240,2240,2232,2232,2232,2232,2224,2224,2224,2224,2216,2216,2216,2216,2208,2208,2208,2208,2200,2200,2200,2200,2192,2192,2192,2192,2184,2184,2184,2184,2176,2176,2176,2176,2168,2168,2168,2168,2160,2160,2160,2160,2152,2152,2152,2152,2144,2144,2144,2144,2136,2136,2136,2136,2128,2128,2128,2128,2120,2120,2120,2120,2112,2112,2112,2112,2104,2104,2104,2104,2096,2096,2096,2096,2088,2088,2088,2088,2080,2080,2080,2080,2072,2072,2072,2072,2064,2064,2064,2064,2056,2056,2056,2056,2048,2048,2044,2044,2048,2048,2044,2044,2044,1916,1916,1916,1912,1912,2036,2036,2036,1908,1908,1908,1904,1904,2028,2028,2028,1900,1900,1900,1896,1896,2020,2020,2020,1892,1892,1892,1888,1888,2012,2012,2012,1884,1884,1884,1880,1880,2004,2004,2004,1876,1876,1876,1872,1872,1996,1996,1996,1868,1868,1868,1864,1864,1988,1988,1988,1860,1860,1860,1856,1856,1980,1980,1980,1852,1852,1852,1848,1848,1972,1972,1972,1844,1844,1844,1840,1840,1964,1964,1964,1836,1836,1836,1832,1832,1956,1956,1956,1828,1828,1828,1824,1824,1948,1948,1948,1820,1820,1820,1816,1816,1940,1940,1940,1812,1812,1812,1808,1808,1932,1932,1932,1804,1804,1804,1800,1800,1924,1924,1924,1796,1796,1796,1792,1792,1788,1788,1788,1660,1660,1660,1656,1656,1780,1780,1780,1652,1652,1652,1648,1648,1772,1772,1772,1644,1644,1644,1640,1640,1764,1764,1764,1636,1636,1636,1632,1632,1756,1756,1756,1628,1628,1628,1624,1624,1748,1748,1748,1620,1620,1620,1616,1616,1740,1740,1740,1612,1612,1612,1608,1608,1732,1732,1732,1604,1604,1604,1600,1600,1724,1724,1724,1596,1596,1596,1592,1592,1716,1716,1716,1588,1588,1588,1584,1584,1708,1708,1708,1580,1580,1580,1576,1576,1700,1700,1700,1572,1572,1572,1568,1568,1692,1692,1692,1564,1564,1564,1560,1560,1684,1684,1684,1556,1556,1556,1552,1552,1676,1676,1676,1548,1548,1548,1544,1544,1668,1668,1668,1540,1540,1540,1536,1532,1404,1404,1404,1276,1148,1148,1144,1524,1396,1396,1396,1268,1140,1140,1136,1516,1388,1388,1388,1260,1132,1132,1128,1508,1380,1380,1380,1252,1124,1124,1120,1500,1372,1372,1372,1244,1116,1116,1112,1492,1364,1364,1364,1236,1108,1108,1104,1484,1356,1356,1356,1228,1100,1100,1096,1476,1348,1348,1348,1220,1092,1092,1088,1468,1340,1340,1340,1212,1084,1084,1080,1460,1332,1332,1332,1204,1076,1076,1072,1452,1324,1324,1324,1196,1068,1068,1064,1444,1316,1316,1316,1188,1060,1060,1056,1436,1308,1308,1308,1180,1052,1052,1048,1428,1300,1300,1300,1172,1044,1044,1040,1420,1292,1292,1292,1164,1036,1036,1032,1412,1284,1284,1284,1156,1028,1028,1020,892,764,636,508,380,252,124,1012,884,756,628,500,372,244,116,1004,876,748,620,492,364,236,108,996,868,740,612,484,356,228,100,988,860,732,604,476,348,220,92,980,852,724,596,468,340,212,84,972,844,716,588,460,332,204,76,964,836,708,580,452,324,196,68,956,828,700,572,444,316,188,60,948,820,692,564,436,308,180,52,940,812,684,556,428,300,172,44,932,804,676,548,420,292,164,36,924,796,668,540,412,284,156,28,916,788,660,532,404,276,148,20,908,780,652,524,396,268,140,12,900,772,644,516,388,260,132,4,1916,1916,2040,2040,2040,1912,1912,1912,1908,1908,2032,2032,2032,1904,1904,1904,1900,1900,2024,2024,2024,1896,1896,1896,1892,1892,2016,2016,2016,1888,1888,1888,1884,1884,2008,2008,2008,1880,1880,1880,1876,1876,2000,2000,2000,1872,1872,1872,1868,1868,1992,1992,1992,1864,1864,1864,1860,1860,1984,1984,1984,1856,1856,1856,1852,1852,1976,1976,1976,1848,1848,1848,1844,1844,1968,1968,1968,1840,1840,1840,1836,1836,1960,1960,1960,1832,1832,1832,1828,1828,1952,1952,1952,1824,1824,1824,1820,1820,1944,1944,1944,1816,1816,1816,1812,1812,1936,1936,1936,1808,1808,1808,1804,1804,1928,1928,1928,1800,1800,1800,1796,1796,1920,1920,1920,1792,1792,1792,1660,1660,1784,1784,1784,1656,1656,1656,1652,1652,1776,1776,1776,1648,1648,1648,1644,1644,1768,1768,1768,1640,1640,1640,1636,1636,1760,1760,1760,1632,1632,1632,1628,1628,1752,1752,1752,1624,1624,1624,1620,1620,1744,1744,1744,1616,1616,1616,1612,1612,1736,1736,1736,1608,1608,1608,1604,1604,1728,1728,1728,1600,1600,1600,1596,1596,1720,1720,1720,1592,1592,1592,1588,1588,1712,1712,1712,1584,1584,1584,1580,1580,1704,1704,1704,1576,1576,1576,1572,1572,1696,1696,1696,1568,1568,1568,1564,1564,1688,1688,1688,1560,1560,1560,1556,1556,1680,1680,1680,1552,1552,1552,1548,1548,1672,1672,1672,1544,1544,1544,1540,1540,1664,1664,1664,1536,1536,1536,1148,1528,1400,1400,1400,1272,1144,1144,1140,1520,1392,1392,1392,1264,1136,1136,1132,1512,1384,1384,1384,1256,1128,1128,1124,1504,1376,1376,1376,1248,1120,1120,1116,1496,1368,1368,1368,1240,1112,1112,1108,1488,1360,1360,1360,1232,1104,1104,1100,1480,1352,1352,1352,1224,1096,1096,1092,1472,1344,1344,1344,1216,1088,1088,1084,1464,1336,1336,1336,1208,1080,1080,1076,1456,1328,1328,1328,1200,1072,1072,1068,1448,1320,1320,1320,1192,1064,1064,1060,1440,1312,1312,1312,1184,1056,1056,1052,1432,1304,1304,1304,1176,1048,1048,1044,1424,1296,1296,1296,1168,1040,1040,1036,1416,1288,1288,1288,1160,1032,1032,1028,1408,1280,1280,1280,1152,1024,1024,1016,888,760,632,504,376,248,120,1008,880,752,624,496,368,240,112,1000,872,744,616,488,360,232,104,992,864,736,608,480,352,224,96,984,856,728,600,472,344,216,88,976,848,720,592,464,336,208,80,968,840,712,584,456,328,200,72,960,832,704,576,448,320,192,64,952,824,696,568,440,312,184,56,944,816,688,560,432,304,176,48,936,808,680,552,424,296,168,40,928,800,672,544,416,288,160,32,920,792,664,536,408,280,152,24,912,784,656,528,400,272,144,16,904,776,648,520,392,264,136,8,896,768,640,512,384,256,128,0,1912,1912,2036,2036,1904,1904,2028,2028,1896,1896,2020,2020,1888,1888,2012,2012,1880,1880,2004,2004,1872,1872,1996,1996,1864,1864,1988,1988,1856,1856,1980,1980,1848,1848,1972,1972,1840,1840,1964,1964,1832,1832,1956,1956,1824,1824,1948,1948,1816,1816,1940,1940,1808,1808,1932,1932,1800,1800,1924,1924,1792,1792,1788,1788,1656,1656,1780,1780,1648,1648,1772,1772,1640,1640,1764,1764,1632,1632,1756,1756,1624,1624,1748,1748,1616,1616,1740,1740,1608,1608,1732,1732,1600,1600,1724,1724,1592,1592,1716,1716,1584,1584,1708,1708,1576,1576,1700,1700,1568,1568,1692,1692,1560,1560,1684,1684,1552,1552,1676,1676,1544,1544,1668,1668,1536,1532,1404,1404,1144,1524,1396,1396,1136,1516,1388,1388,1128,1508,1380,1380,1120,1500,1372,1372,1112,1492,1364,1364,1104,1484,1356,1356,1096,1476,1348,1348,1088,1468,1340,1340,1080,1460,1332,1332,1072,1452,1324,1324,1064,1444,1316,1316,1056,1436,1308,1308,1048,1428,1300,1300,1040,1420,1292,1292,1032,1412,1284,1284,1020,892,764,636,1012,884,756,628,1004,876,748,620,996,868,740,612,988,860,732,604,980,852,724,596,972,844,716,588,964,836,708,580,956,828,700,572,948,820,692,564,940,812,684,556,932,804,676,548,924,796,668,540,916,788,660,532,908,780,652,524,900,772,644,516,2816,2816,2816,2816};

__device__ __forceinline__ float lo(unsigned v){ return __uint_as_float(v<<16); }
__device__ __forceinline__ float hi(unsigned v){ return __uint_as_float(v&0xffff0000u); }
typedef __attribute__((ext_vector_type(2))) __bf16 bf2;
typedef __attribute__((ext_vector_type(2))) float f2;
typedef __attribute__((ext_vector_type(4))) float f4;
__device__ __forceinline__ unsigned pk2(f2 a){ return __builtin_bit_cast(unsigned,__builtin_convertvector(a,bf2)); }
__device__ __forceinline__ unsigned pk(float a,float b){ f2 v={a,b}; return pk2(v); }
__device__ __forceinline__ float rbf(float a){ return hi(pk(a,a)); }
__device__ __forceinline__ float dot2(unsigned a,unsigned b,float c){ return __builtin_amdgcn_fdot2_f32_bf16(__builtin_bit_cast(bf2,a),__builtin_bit_cast(bf2,b),c,false); }
__device__ __forceinline__ float red8(float s){
  s+=__builtin_amdgcn_update_dpp(0.f,s,0xB1,0xf,0xf,true); s+=__builtin_amdgcn_update_dpp(0.f,s,0x4E,0xf,0xf,true); s+=__builtin_amdgcn_update_dpp(0.f,s,0x141,0xf,0xf,true); return s; }
#define RLX __ATOMIC_RELAXED, __HIP_MEMORY_SCOPE_AGENT

__device__ __forceinline__ void helper(const u16* x,const u16* wg,const u16* wu,const u16* wd,char* ws,char* sm,int b,int w,int l){
  const int tid=w*64+l;
#define ST(x)
  const int g=b&7, hi_=(b>>3)-28, gm=g&3, gf=g>>2;
  const int th=8*gm+4*gf+hi_, uu=4*gm+hi_;
  const __amdgpu_buffer_rsrc_t rs=__builtin_amdgcn_make_buffer_rsrc(ws,0,WS_SIZE,0x20000);
  const u64 tag=__hip_atomic_load((gu64*)(ws+WS_E),RLX)+0x7F4A7C159E3779B9ull;
  unsigned cp_=0;
  if(w==7){ unsigned lo_,hi2,n;
    asm volatile("s_getpc_b64 s[20:21]\n.Lpc:\n s_sub_u32 %0, s20, .Lpc-.Lstart\n s_subb_u32 %1, s21, 0\n s_mov_b32 %2, .Lend-.Lstart":"=s"(lo_),"=s"(hi2),"=s"(n)::"scc","s20","s21");
    const unsigned o=(hi_*64+l)*128; const u64 pc=((u64)hi2<<32)|lo_;
    asm volatile("global_load_dword %0, %1, %2":"=v"(cp_):"v"(o<n-4?o:0u),"s"(pc):"memory"); }
  u32x4 xv[7], wv[14];
  const char* xp=(const char*)x+(size_t)(64*th+8*w+(l>>3))*896+(l&7)*16;
#pragma unroll
  for(int i=0;i<7;i++) xv[i]=*(const g32x4*)(xp+i*128);
  if(XB==1) asm volatile("s_barrier":::"memory");
  if(XB==2) asm volatile("s_waitcnt vmcnt(2)":::"memory");
  __builtin_amdgcn_sched_barrier(0);
  const bool cs=uu<14; const int j=7*gf+(cs?uu>>1:6), m=uu&1;
  { const char* wp=(const char*)(m?wu:wg)+(size_t)(56*w+(l>>4))*3584+j*256+(l&15)*16;
#pragma unroll
    for(int i=0;i<14;i++) wv[i]=*(const g32x4*)(wp+(size_t)i*4*3584);
  }
  { constexpr int rg=0;
    float s=0.f, s1=0.f; f2 v[28];
#pragma unroll
    for(int i=0;i<28;i+=2){ s=dot2(xv[7*rg+i/4][i&3],0x3f803f80u,s); s1=dot2(xv[7*rg+i/4][(i&3)+1],0x3f803f80u,s1); }
    const float mean=red8(s+s1)*(1.f/448.f); f2 q2={0.f,0.f}, q3={0.f,0.f};
#pragma unroll
    for(int i=0;i<28;i++){ const unsigned u=xv[7*rg+i/4][i&3]; f2 d={lo(u),hi(u)}; d-=mean; v[i]=d; if(i&1) q3+=d*d; else q2+=d*d; }
    q2+=q3;
    const float mb=rbf(mean), sd=rbf(__builtin_amdgcn_sqrtf(rbf(rbf(red8(q2[0]+q2[1])*(1.f/448.f))+1e-5f))), r=__builtin_amdgcn_rcpf(sd), c0=(mean-mb)*r;
    const int row=8*w+(l>>3);
    if((l&7)==0){
      u32x2 o; o[0]=pk(-mb,-mb); o[1]=(unsigned)tag;
      __builtin_amdgcn_raw_buffer_store_b64(o,rs,WS_STM+(64*th+row)*8,0,16);
      __builtin_amdgcn_raw_buffer_store_b32(__float_as_uint(r*r),rs,WS_STR+(64*th+row)*4,0,16); }
#pragma unroll
    for(int i=0;i<7;i++){ u32x4 o;
#pragma unroll
      for(int q=0;q<4;q++) o[q]=pk2(v[4*i+q]*r+c0);
      *(u32x4*)(sm+row*912+i*128+(l&7)*16)=o; }
  }
  asm volatile("s_waitcnt lgkmcnt(0)\n s_barrier"::"v"(cp_):"memory"); ST(T1);
  char* s2=sm+58368; const int G=l>>4, q=(l>>2)&3, p=l&3;
#pragma unroll
  for(int c=0;c<7;c++){ const int cg=7*(w>>1)+c, h=w&1, t0=32*h+8*G;
    const s16x4 a=__builtin_amdgcn_ds_read_tr16_b64_v4i16((lds_s16x4*)(sm+(t0+q)*912+cg*32+p*8));
    const s16x4 bb=__builtin_amdgcn_ds_read_tr16_b64_v4i16((lds_s16x4*)(sm+(t0+4+q)*912+cg*32+p*8));
    u32x4 o; const u32x2 a2=__builtin_bit_cast(u32x2,a), b2=__builtin_bit_cast(u32x2,bb); o[0]=a2[0];o[1]=a2[1];o[2]=b2[0];o[3]=b2[1];
    *(u32x4*)(s2+(16*cg+(l&15))*144+(4*h+G)*16)=o; }
  asm volatile("s_waitcnt lgkmcnt(0)\n s_barrier":::"memory");
#pragma unroll
  for(int it=0;it<7;it++){ const u32x4 uo=*(const u32x4*)(s2+(56*w+8*it+(l>>3))*144+(l&7)*16);
    __builtin_amdgcn_raw_buffer_store_b128(uo,rs,WS_UT+(56*w+8*it+(l>>3))*US+th*128+(l&7)*16,0,16); }
  ST(T2);
  __builtin_amdgcn_sched_barrier(0);
  { f4 a0={0.f,0.f,0.f,0.f}, a1={0.f,0.f,0.f,0.f}; unsigned e0=0x00003f80u,e1=0x3f800000u; asm volatile("":"+v"(e0),"+v"(e1));
#pragma unroll
    for(int i=0;i<14;i++){
#pragma unroll
      for(int k=0;k<2;k++){ a0[2*k]=dot2(wv[i][k],e0,a0[2*k]); a0[2*k+1]=dot2(wv[i][k],e1,a0[2*k+1]);
                            a1[2*k]=dot2(wv[i][2+k],e0,a1[2*k]); a1[2*k+1]=dot2(wv[i][2+k],e1,a1[2*k+1]); } }
    f4* rd=(f4*)(sm+131072+tid*32); rd[0]=a0; rd[1]=a1; }
  asm volatile("s_waitcnt vmcnt(0) lgkmcnt(0)\n s_barrier":::"memory");
  if(tid<8) __hip_atomic_store((gu64*)(ws+WS_FHLP+tid*16384+128*th),tag,RLX);
  ST(T3);
  {
    const float* rd=(const float*)(sm+131072)+16*w+(l&15)+(l>>4)*1024; float c=0.f;
#pragma unroll
    for(int i=0;i<8;i++) c+=rd[128*i];
    c+=__shfl_xor(c,16); c+=__shfl_xor(c,32);
    u32x2 o; o[0]=__float_as_uint(c); o[1]=(unsigned)tag;
    if(cs&&l<16) __builtin_amdgcn_raw_buffer_store_b64(o,rs,WS_CS+j*2048+w*256+(16*m+l)*8,0,16); }
  if(b==251){
    gu64* f=(gu64*)(ws+(tid<224 ? WS_FH+(((tid/14)*2)*32+(tid%14)*2)*128 : WS_FHLP+3*16384+((tid-224)&31)*128));
    __builtin_amdgcn_s_sleep(127);
    for(int it=0;it<SPIN_LIMIT;it++){ if(__hip_atomic_load(f,RLX)==tag) break; __builtin_amdgcn_s_sleep(ESLP); }
    __syncthreads();
    if(tid==0) __hip_atomic_store((gu64*)(ws+WS_E),tag,RLX);
  }
}

__global__ __launch_bounds__(512,2) void fused_kernel(const u16* x,const u16* lar,const u16* aim,const u16* cr,const u16* ci,const u16* ldt,const u16* dsk,
                                                     const u16* wg,const u16* wu,const u16* wd,u16* out,char* ws){
  __shared__ __attribute__((aligned(16))) char sm[163840];
  typedef const char* const __attribute__((address_space(4)))* kptr;
  kptr kp=(kptr)__builtin_amdgcn_kernarg_segment_ptr();
  int b=(blockIdx.x+HOFF)&255, w=__builtin_amdgcn_readfirstlane(threadIdx.x>>6);
  asm volatile(
".Lstart:\n"
"s_cmp_ge_u32 %1, 0xe0\n"
"s_cbranch_scc1 .Lhelp\n"
"s_load_dwordx16 s[16:31], %0, 0x0\n"
"s_load_dwordx2 s[34:35], %0, 0x40\n"
"s_load_dwordx2 s[52:53], %0, 0x58\n"
"s_load_dwordx2 s[48:49], %0, 0x48\n"
"s_load_dwordx2 s[56:57], %0, 0x50\n"
"s_mov_b32 s96, %1\n"
"s_mov_b32 s81, %2\n"
"s_mov_b64 s[98:99], %3\n"
"v_mbcnt_lo_u32_b32 v232, -1, 0\n"
"v_mbcnt_hi_u32_b32 v232, -1, v232\n"
"s_and_b32 s97, s81, 3\n"
"s_and_b32 s66, s97, 1\n"
"s_lshr_b32 s67, s97, 1\n"
"v_mov_b32 v233, 0\n"
"v_mov_b32 v249, 1\n"
"v_lshlrev_b32 v251, 2, v232\n"
"v_min_u32 v251, 60, v251\n"
"v_add_u32 v251, 0x27e40, v251\n"
"ds_write_b32 v251, v233\n"
"s_and_b32 s71, s96, 7\n"
"s_lshr_b32 s72, s96, 3\n"
"s_and_b32 s73, s71, 3\n"
"s_and_b32 s74, s72, 3\n"
"s_lshr_b32 s65, s72, 2\n"
"s_lshl_b32 s62, s73, 2\n"
"s_add_u32 s62, s62, s74\n"
"s_lshr_b32 s63, s71, 2\n"
"s_mul_i32 s63, s63, 7\n"
"s_add_u32 s63, s63, s65\n"
"s_lshl_b32 s64, s71, 2\n"
"s_add_u32 s64, s64, s74\n"
"s_mov_b32 s39, 0xffff0000\n"
"s_cmp_ge_u32 s81, 4\n"
"s_cbranch_scc1 .Lswave\n"
"s_lshl_b32 s79, s97, 12\n"
"s_add_u32 s79, s79, 0x10000\n"
"s_lshl_b32 s78, s66, 13\n"
"s_lshl_b32 s77, s67, 12\n"
"s_add_u32 s78, s78, s77\n"
"s_lshl_b32 s75, s62, 7\n"
"s_lshl_b32 s77, s97, 5\n"
"s_add_u32 s75, s75, s77\n"
"v_lshrrev_b32 v251, 3, v232\n"
"v_add_u32 v251, s75, v251\n"
"v_mul_u32_u24 v251, 0x380, v251\n"
"v_lshrrev_b32 v252, 4, v232\n"
"v_and_b32 v253, 7, v232\n"
"v_xor_b32 v253, v253, v252\n"
"v_lshlrev_b32 v253, 4, v253\n"
"v_add_u32 v216, v251, v253\n"
"v_xor_b32 v253, 64, v253\n"
"v_add_u32 v253, 0x1800, v253\n"
"v_add_u32 v217, v251, v253\n"
"v_add_u32 v218, 0x3000, v216\n"
"v_add_u32 v219, 0x3000, v217\n"
"s_mul_i32 s75, s67, 0xe000\n"
"s_lshl_b32 s77, s63, 8\n"
"s_add_u32 s75, s75, s77\n"
"s_lshl_b32 s76, s66, 1\n"
"v_bfe_u32 v251, v232, 2, 2\n"
"v_mul_u32_u24 v251, 0xe00, v251\n"
"v_lshl_add_u32 v251, v252, 6, v251\n"
"v_and_b32 v253, 3, v232\n"
"v_xor_b32 v253, s76, v253\n"
"v_lshl_add_u32 v251, v253, 4, v251\n"
"v_add_u32 v220, s75, v251\n"
"v_add_u32 v221, 0x3400, v220\n"
"v_add_u32 v222, 0x6800, v220\n"
"v_add_u32 v223, 0x9c00, v220\n"
"s_waitcnt lgkmcnt(0)\n"
"s_mov_b32 s40, s16\n"
"s_and_b32 s41, s17, 0xffff\n"
"s_mov_b32 s42, 0x1c0000\n"
"s_mov_b32 s43, 0x20000\n"
"s_cmp_eq_u32 s66, 0\n"
"s_cselect_b32 s44, s30, s34\n"
"s_cselect_b32 s45, s31, s35\n"
"s_and_b32 s45, s45, 0xffff\n"
"s_mov_b32 s46, 0x188000\n"
"s_mov_b32 s47, 0x20000\n"
"s_mov_b32 s82, 0\n"
"s_add_u32 m0, s78, 0\n"
"s_nop 0\n"
"buffer_load_dwordx4 v220, s[44:47], s82 offen lds\n"
"buffer_load_dwordx4 v221, s[44:47], s82 offen offset:1024 lds\n"
"buffer_load_dwordx4 v222, s[44:47], s82 offen offset:2048 lds\n"
"buffer_load_dwordx4 v223, s[44:47], s82 offen offset:3072 lds\n"
"s_mov_b32 s83, 0\n"
"s_add_u32 m0, s79, 0\n"
"s_nop 0\n"
"buffer_load_dwordx4 v216, s[40:43], s83 offen lds\n"
"buffer_load_dwordx4 v217, s[40:43], s83 offen offset:1024 lds\n"
"buffer_load_dwordx4 v218, s[40:43], s83 offen offset:2048 lds\n"
"buffer_load_dwordx4 v219, s[40:43], s83 offen offset:3072 lds\n"
"s_mov_b32 s82, 0x1c000\n"
"s_add_u32 m0, s78, 0x4000\n"
"s_nop 0\n"
"buffer_load_dwordx4 v220, s[44:47], s82 offen lds\n"
"buffer_load_dwordx4 v221, s[44:47], s82 offen offset:1024 lds\n"
"buffer_load_dwordx4 v222, s[44:47], s82 offen offset:2048 lds\n"
"buffer_load_dwordx4 v223, s[44:47], s82 offen offset:3072 lds\n"
"s_barrier\n"
"s_and_b32 s53, s53, 0xffff\n"
"s_mov_b32 s54, 0xbe3000\n"
"s_mov_b32 s55, 0x20000\n"
"s_and_b32 s57, s57, 0xffff\n"
"s_mov_b32 s58, 0x1c0000\n"
"s_mov_b32 s59, 0x20000\n"
"s_and_b32 s49, s49, 0xffff\n"
"s_mov_b32 s50, 0x188000\n"
"s_mov_b32 s51, 0x20000\n"
"buffer_load_dwordx2 v[234:235], v233, s[52:55], 0 offen sc1\n"
"v_mov_b32 v248, 0x27e40\n"
"s_mov_b32 s82, 0x38000\n"
"s_add_u32 m0, s78, 0x8000\n"
"s_nop 0\n"
"buffer_load_dwordx4 v220, s[44:47], s82 offen lds\n"
"buffer_load_dwordx4 v221, s[44:47], s82 offen offset:1024 lds\n"
"buffer_load_dwordx4 v222, s[44:47], s82 offen offset:2048 lds\n"
"buffer_load_dwordx4 v223, s[44:47], s82 offen offset:3072 lds\n"
"s_mov_b32 s83, 0x80\n"
"s_add_u32 m0, s79, 0x4000\n"
"s_nop 0\n"
"buffer_load_dwordx4 v216, s[40:43], s83 offen lds\n"
"buffer_load_dwordx4 v217, s[40:43], s83 offen offset:1024 lds\n"
"buffer_load_dwordx4 v218, s[40:43], s83 offen offset:2048 lds\n"
"buffer_load_dwordx4 v219, s[40:43], s83 offen offset:3072 lds\n"
"s_mov_b32 s82, 0x54000\n"
"s_add_u32 m0, s78, 0xc000\n"
"s_nop 0\n"
"buffer_load_dwordx4 v220, s[44:47], s82 offen lds\n"
"buffer_load_dwordx4 v221, s[44:47], s82 offen offset:1024 lds\n"
"buffer_load_dwordx4 v222, s[44:47], s82 offen offset:2048 lds\n"
"buffer_load_dwordx4 v223, s[44:47], s82 offen offset:3072 lds\n"
"v_lshrrev_b32 v254, 5, v232\n"
"v_bfe_u32 v255, v232, 2, 2\n"
"v_and_b32 v208, 3, v232\n"
"v_bfe_u32 v209, v232, 4, 1\n"
"s_lshl_b32 s75, s67, 9\n"
"v_lshlrev_b32 v251, 13, v209\n"
"v_lshl_add_u32 v251, v254, 11, v251\n"
"v_lshl_add_u32 v251, v255, 6, v251\n"
"v_lshl_add_u32 v251, v209, 5, v251\n"
"v_lshl_add_u32 v251, v208, 3, v251\n"
"v_add_u32 v230, s75, v251\n"
"v_xor_b32 v231, 32, v230\n"
"s_lshl_b32 s75, s66, 13\n"
"s_add_u32 s75, s75, 0x10000\n"
"v_bfe_u32 v251, v232, 1, 3\n"
"v_xor_b32 v251, v251, v254\n"
"v_lshlrev_b32 v251, 4, v251\n"
"v_and_b32 v252, 31, v232\n"
"v_lshl_add_u32 v251, v252, 7, v251\n"
"v_add_u32 v226, s75, v251\n"
"v_xor_b32 v227, 32, v226\n"
"v_xor_b32 v228, 64, v226\n"
"v_xor_b32 v229, 0x60, v226\n"
"s_waitcnt vmcnt(13)\n"
"s_mov_b64 exec, 1\n"
"ds_add_u32 v248, v249\n"
"s_mov_b64 exec, -1\n"
"ds_read_b32 v250, v248\n"
"s_waitcnt lgkmcnt(0)\n"
"v_cmp_gt_u32 vcc, 4, v250\n"
"s_cbranch_vccnz .Lslow_c1\n"
".Lret_c1:\n"
"s_cmp_eq_u32 s66, 0\n"
"s_cbranch_scc1 .Lst0\n"
"s_nop 3\n"
".Lst0:\n"
"s_cmp_eq_u32 s67, 0\n"
"s_cbranch_scc1 .Lst1\n"
"s_nop 7\n"
".Lst1:\n"
"ds_read_b64_tr_b16 v[160:161], v230\n"
"ds_read_b64_tr_b16 v[162:163], v230 offset:1024\n"
"ds_read_b64_tr_b16 v[164:165], v231\n"
"ds_read_b64_tr_b16 v[166:167], v231 offset:1024\n"
"ds_read_b64_tr_b16 v[168:169], v230 offset:256\n"
"ds_read_b64_tr_b16 v[170:171], v230 offset:1280\n"
"ds_read_b64_tr_b16 v[172:173], v231 offset:256\n"
"ds_read_b64_tr_b16 v[174:175], v231 offset:1280\n"
"ds_read_b128 v[128:131], v226\n"
"ds_read_b128 v[132:135], v226 offset:4096\n"
"ds_read_b128 v[136:139], v227\n"
"ds_read_b128 v[140:143], v227 offset:4096\n"
"s_waitcnt lgkmcnt(3)\n"
"v_mfma_f32_32x32x16_bf16 v[0:15], v[160:163], v[128:131], 0\n"
"ds_read_b64_tr_b16 v[176:177], v230 offset:4096\n"
"ds_read_b64_tr_b16 v[178:179], v230 offset:5120\n"
"s_waitcnt vmcnt(12)\n"
"v_readfirstlane_b32 s60, v234\n"
"v_readfirstlane_b32 s61, v235\n"
"s_add_u32 s60, s60, 0x9E3779B9\n"
"s_addc_u32 s61, s61, 0x7F4A7C15\n"
"v_mov_b32 v238, s60\n"
"v_mov_b32 v239, s61\n"
"s_waitcnt lgkmcnt(4)\n"
"v_mfma_f32_32x32x16_bf16 v[64:79], v[160:163], v[132:135], 0\n"
"ds_read_b64_tr_b16 v[180:181], v231 offset:4096\n"
"ds_read_b64_tr_b16 v[182:183], v231 offset:5120\n"
"s_and_b32 s75, s64, 3\n"
"s_lshl_b32 s69, s75, 1\n"
"s_add_u32 s69, s69, s65\n"
"s_lshr_b32 s75, s64, 2\n"
"s_add_u32 s69, s69, s75\n"
"v_mfma_f32_32x32x16_bf16 v[16:31], v[164:167], v[128:131], 0\n"
"ds_read_b64_tr_b16 v[184:185], v230 offset:4352\n"
"ds_read_b64_tr_b16 v[186:187], v230 offset:5376\n"
"s_mul_i32 s75, s69, 37\n"
"s_lshr_b32 s75, s75, 8\n"
"s_mul_i32 s75, s75, 7\n"
"s_sub_u32 s69, s69, s75\n"
"s_mul_i32 s75, s69, 1\n"
"v_mfma_f32_32x32x16_bf16 v[80:95], v[164:167], v[132:135], 0\n"
"ds_read_b64_tr_b16 v[188:189], v231 offset:4352\n"
"ds_read_b64_tr_b16 v[190:191], v231 offset:5376\n"
"v_add_u32 v251, s75, v232\n"
"v_subrev_u32 v252, 7, v251\n"
"v_min_u32 v251, v251, v252\n"
"v_mul_u32_u24 v240, 0x1000, v251\n"
"s_mul_i32 s75, s69, 4\n"
"v_mfma_f32_32x32x16_bf16 v[32:47], v[168:171], v[128:131], 0\n"
"ds_read_b64_tr_b16 v[192:193], v230 offset:16384\n"
"ds_read_b64_tr_b16 v[194:195], v230 offset:17408\n"
"v_add_u32 v251, s75, v232\n"
"v_subrev_u32 v252, 28, v251\n"
"v_min_u32 v251, v251, v252\n"
"v_mul_u32_u24 v241, 0x3800, v251\n"
"s_mul_i32 s75, s97, 0x62000\n"
"v_mfma_f32_32x32x16_bf16 v[96:111], v[168:171], v[132:135], 0\n"
"ds_read_b64_tr_b16 v[196:197], v231 offset:16384\n"
"ds_read_b64_tr_b16 v[198:199], v231 offset:17408\n"
"s_lshl_b32 s77, s65, 7\n"
"s_add_u32 s75, s75, s77\n"
"v_lshrrev_b32 v251, 3, v232\n"
"v_mul_u32_u24 v251, 0x380, v251\n"
"v_and_b32 v252, 7, v232\n"
"v_mfma_f32_32x32x16_bf16 v[48:63], v[172:175], v[128:131], 0\n"
"ds_read_b64_tr_b16 v[200:201], v230 offset:16640\n"
"ds_read_b64_tr_b16 v[202:203], v230 offset:17664\n"
"v_bfe_u32 v254, v232, 4, 1\n"
"v_lshlrev_b32 v253, 2, v254\n"
"v_xor_b32 v252, v252, v253\n"
"v_lshl_add_u32 v251, v252, 4, v251\n"
"v_mfma_f32_32x32x16_bf16 v[112:127], v[172:175], v[132:135], 0\n"
"ds_read_b64_tr_b16 v[204:205], v231 offset:16640\n"
"ds_read_b64_tr_b16 v[206:207], v231 offset:17664\n"
"v_add_u32 v242, s75, v251\n"
"v_add_u32 v243, 0x1800, v242\n"
"v_lshrrev_b32 v251, 5, v232\n"
"v_bfe_u32 v252, v232, 2, 2\n"
"s_waitcnt lgkmcnt(2)\n"
"v_mfma_f32_32x32x16_bf16 v[0:15], v[176:179], v[136:139], v[0:15]\n"
"ds_read_b128 v[144:147], v228\n"
"ds_read_b128 v[148:151], v228 offset:4096\n"
"v_lshl_add_u32 v251, v251, 2, v252\n"
"v_lshlrev_b32 v251, 7, v251\n"
"v_lshrrev_b32 v252, 1, v252\n"
"v_lshl_add_u32 v251, v252, 6, v251\n"
"v_mfma_f32_32x32x16_bf16 v[64:79], v[176:179], v[140:143], v[64:79]\n"
"ds_read_b128 v[152:155], v229\n"
"ds_read_b128 v[156:159], v229 offset:4096\n"
"v_lshl_add_u32 v251, v254, 5, v251\n"
"v_and_b32 v252, 3, v232\n"
"v_lshl_add_u32 v251, v252, 3, v251\n"
"s_lshl_b32 s80, s97, 12\n"
"v_add_u32 v244, s80, v251\n"
"v_mfma_f32_32x32x16_bf16 v[16:31], v[180:183], v[136:139], v[16:31]\n"
"ds_read_b64_tr_b16 v[160:161], v230 offset:20480\n"
"ds_read_b64_tr_b16 v[162:163], v230 offset:21504\n"
"ds_read_b64_tr_b16 v[164:165], v231 offset:20480\n"
"ds_read_b64_tr_b16 v[166:167], v231 offset:21504\n"
"ds_read_b64_tr_b16 v[168:169], v230 offset:20736\n"
"ds_read_b64_tr_b16 v[170:171], v230 offset:21760\n"
"ds_read_b64_tr_b16 v[172:173], v231 offset:20736\n"
"ds_read_b64_tr_b16 v[174:175], v231 offset:21760\n"
"v_mfma_f32_32x32x16_bf16 v[80:95], v[180:183], v[140:143], v[80:95]\n"
"v_xor_b32 v245, 64, v244\n"
"s_mul_i32 s75, s62, 0x70200\n"
"s_mul_i32 s77, s66, 0x38100\n"
"s_add_u32 s75, s75, s77\n"
"s_lshl_b32 s77, s63, 13\n"
"s_add_u32 s75, s75, s77\n"
"s_lshl_b32 s77, s67, 12\n"
"v_mfma_f32_32x32x16_bf16 v[32:47], v[184:187], v[136:139], v[32:47]\n"
"s_add_u32 s75, s75, s77\n"
"s_add_u32 s75, s75, 0x400000\n"
"v_lshl_add_u32 v246, v232, 4, s75\n"
"s_mov_b32 s91, 0x1c080\n"
"s_lshl_b32 s75, s63, 11\n"
"s_lshl_b32 s77, s67, 10\n"
"s_add_u32 s75, s75, s77\n"
"v_mfma_f32_32x32x16_bf16 v[96:111], v[184:187], v[140:143], v[96:111]\n"
"s_add_u32 s75, s75, 0x8400\n"
"v_and_b32 v251, 31, v232\n"
"v_lshl_add_u32 v236, v251, 3, s75\n"
"s_lshl_b32 s75, s62, 10\n"
"s_lshl_b32 s77, s66, 9\n"
"s_add_u32 s75, s75, s77\n"
"s_add_u32 s75, s75, 0x10000\n"
"v_mfma_f32_32x32x16_bf16 v[48:63], v[188:191], v[136:139], v[48:63]\n"
"s_waitcnt lgkmcnt(0)\n"
"s_waitcnt vmcnt(4)\n"
"s_mov_b64 exec, 1\n"
"ds_add_u32 v248, v249\n"
"s_mov_b64 exec, -1\n"
"v_lshl_add_u32 v235, v251, 3, s75\n"
"v_mfma_f32_32x32x16_bf16 v[112:127], v[188:191], v[140:143], v[112:127]\n"
"v_mov_b32 v210, 0\n"
"v_mov_b32 v211, 0\n"
"v_mov_b32 v214, 0\n"
"v_mov_b32 v215, 0\n"
"s_lshl_b32 s93, s62, 13\n"
"s_lshl_b32 s77, s66, 12\n"
"s_add_u32 s93, s93, s77\n"
"v_mfma_f32_32x32x16_bf16 v[0:15], v[192:195], v[144:147], v[0:15]\n"
"ds_read_b32 v250, v248\n"
"s_lshl_b32 s77, s63, 8\n"
"s_add_u32 s93, s93, s77\n"
"s_lshl_b32 s77, s67, 7\n"
"s_add_u32 s93, s93, s77\n"
"s_add_u32 s93, s93, 0xb23000\n"
"v_mfma_f32_32x32x16_bf16 v[64:79], v[192:195], v[148:151], v[64:79]\n"
"v_mfma_f32_32x32x16_bf16 v[16:31], v[196:199], v[144:147], v[16:31]\n"
"v_mfma_f32_32x32x16_bf16 v[80:95], v[196:199], v[148:151], v[80:95]\n"
"s_waitcnt lgkmcnt(0)\n"
"v_cmp_gt_u32 vcc, 8, v250\n"
"s_cbranch_vccnz .Lslow_c2\n"
".Lret_c2:\n"
"ds_read_b64_tr_b16 v[176:177], v230 offset:32768\n"
"ds_read_b64_tr_b16 v[178:179], v230 offset:33792\n"
"s_mov_b32 s82, 0x70000\n"
"s_add_u32 m0, s78, 0\n"
"v_mfma_f32_32x32x16_bf16 v[32:47], v[200:203], v[144:147], v[32:47]\n"
"ds_read_b64_tr_b16 v[180:181], v231 offset:32768\n"
"ds_read_b64_tr_b16 v[182:183], v231 offset:33792\n"
"buffer_load_dwordx4 v220, s[44:47], s82 offen lds\n"
"v_mfma_f32_32x32x16_bf16 v[96:111], v[200:203], v[148:151], v[96:111]\n"
"ds_read_b64_tr_b16 v[184:185], v230 offset:33024\n"
"ds_read_b64_tr_b16 v[186:187], v230 offset:34048\n"
"buffer_load_dwordx4 v221, s[44:47], s82 offen offset:1024 lds\n"
"v_mfma_f32_32x32x16_bf16 v[48:63], v[204:207], v[144:147], v[48:63]\n"
"ds_read_b64_tr_b16 v[188:189], v231 offset:33024\n"
"ds_read_b64_tr_b16 v[190:191], v231 offset:34048\n"
"v_mfma_f32_32x32x16_bf16 v[112:127], v[204:207], v[148:151], v[112:127]\n"
"ds_read_b64_tr_b16 v[192:193], v230 offset:36864\n"
"ds_read_b64_tr_b16 v[194:195], v230 offset:37888\n"
"buffer_load_dwordx4 v222, s[44:47], s82 offen offset:2048 lds\n"
"v_mfma_f32_32x32x16_bf16 v[0:15], v[160:163], v[152:155], v[0:15]\n"
"ds_read_b64_tr_b16 v[196:197], v231 offset:36864\n"
"ds_read_b64_tr_b16 v[198:199], v231 offset:37888\n"
"v_mfma_f32_32x32x16_bf16 v[64:79], v[160:163], v[156:159], v[64:79]\n"
"ds_read_b64_tr_b16 v[200:201], v230 offset:37120\n"
"ds_read_b64_tr_b16 v[202:203], v230 offset:38144\n"
"buffer_load_dwordx4 v223, s[44:47], s82 offen offset:3072 lds\n"
"v_mfma_f32_32x32x16_bf16 v[16:31], v[164:167], v[152:155], v[16:31]\n"
"ds_read_b64_tr_b16 v[204:205], v231 offset:37120\n"
"ds_read_b64_tr_b16 v[206:207], v231 offset:38144\n"
"ds_read_b128 v[128:131], v226 offset:16384\n"
"ds_read_b128 v[132:135], v226 offset:20480\n"
"v_mfma_f32_32x32x16_bf16 v[80:95], v[164:167], v[156:159], v[80:95]\n"
"ds_read_b128 v[136:139], v227 offset:16384\n"
"ds_read_b128 v[140:143], v227 offset:20480\n"
"s_mov_b32 s82, 0x100\n"
"s_add_u32 m0, s79, 0\n"
"s_nop 0\n"
"buffer_load_dwordx4 v216, s[40:43], s82 offen lds\n"
"v_mfma_f32_32x32x16_bf16 v[32:47], v[168:171], v[152:155], v[32:47]\n"
"ds_read_b128 v[144:147], v228 offset:16384\n"
"ds_read_b128 v[148:151], v228 offset:20480\n"
"v_mfma_f32_32x32x16_bf16 v[96:111], v[168:171], v[156:159], v[96:111]\n"
"buffer_load_dwordx4 v217, s[40:43], s82 offen offset:1024 lds\n"
"v_mfma_f32_32x32x16_bf16 v[48:63], v[172:175], v[152:155], v[48:63]\n"
"s_waitcnt lgkmcnt(4)\n"
"s_waitcnt vmcnt(6)\n"
"s_mov_b64 exec, 1\n"
"ds_add_u32 v248, v249\n"
"s_mov_b64 exec, -1\n"
"ds_read_b128 v[152:155], v229 offset:16384\n"
"v_mfma_f32_32x32x16_bf16 v[112:127], v[172:175], v[156:159], v[112:127]\n"
"ds_read_b128 v[156:159], v229 offset:20480\n"
"buffer_load_dwordx4 v218, s[40:43], s82 offen offset:2048 lds\n"
"v_mfma_f32_32x32x16_bf16 v[0:15], v[176:179], v[128:131], v[0:15]\n"
"ds_read_b32 v250, v248\n"
"v_mfma_f32_32x32x16_bf16 v[64:79], v[176:179], v[132:135], v[64:79]\n"
"buffer_load_dwordx4 v219, s[40:43], s82 offen offset:3072 lds\n"
"v_mfma_f32_32x32x16_bf16 v[16:31], v[180:183], v[128:131], v[16:31]\n"
"v_mfma_f32_32x32x16_bf16 v[80:95], v[180:183], v[132:135], v[80:95]\n"
"s_waitcnt lgkmcnt(0)\n"
"v_cmp_gt_u32 vcc, 12, v250\n"
"s_cbranch_vccnz .Lslow_c3\n"
".Lret_c3:\n"
"ds_read_b64_tr_b16 v[160:161], v230 offset:49152\n"
"ds_read_b64_tr_b16 v[162:163], v230 offset:50176\n"
"s_mov_b32 s82, 0x8c000\n"
"s_add_u32 m0, s78, 0x4000\n"
"v_mfma_f32_32x32x16_bf16 v[32:47], v[184:187], v[128:131], v[32:47]\n"
"ds_read_b64_tr_b16 v[164:165], v231 offset:49152\n"
"ds_read_b64_tr_b16 v[166:167], v231 offset:50176\n"
"buffer_load_dwordx4 v220, s[44:47], s82 offen lds\n"
"v_mfma_f32_32x32x16_bf16 v[96:111], v[184:187], v[132:135], v[96:111]\n"
"ds_read_b64_tr_b16 v[168:169], v230 offset:49408\n"
"ds_read_b64_tr_b16 v[170:171], v230 offset:50432\n"
"buffer_load_dwordx4 v221, s[44:47], s82 offen offset:1024 lds\n"
"v_mfma_f32_32x32x16_bf16 v[48:63], v[188:191], v[128:131], v[48:63]\n"
"ds_read_b64_tr_b16 v[172:173], v231 offset:49408\n"
"ds_read_b64_tr_b16 v[174:175], v231 offset:50432\n"
"v_mfma_f32_32x32x16_bf16 v[112:127], v[188:191], v[132:135], v[112:127]\n"
"ds_read_b64_tr_b16 v[176:177], v230 offset:53248\n"
"ds_read_b64_tr_b16 v[178:179], v230 offset:54272\n"
"buffer_load_dwordx4 v222, s[44:47], s82 offen offset:2048 lds\n"
"v_mfma_f32_32x32x16_bf16 v[0:15], v[192:195], v[136:139], v[0:15]\n"
"ds_read_b64_tr_b16 v[180:181], v231 offset:53248\n"
"ds_read_b64_tr_b16 v[182:183], v231 offset:54272\n"
"v_mfma_f32_32x32x16_bf16 v[64:79], v[192:195], v[140:143], v[64:79]\n"
"ds_read_b64_tr_b16 v[184:185], v230 offset:53504\n"
"ds_read_b64_tr_b16 v[186:187], v230 offset:54528\n"
"buffer_load_dwordx4 v223, s[44:47], s82 offen offset:3072 lds\n"
"v_mfma_f32_32x32x16_bf16 v[16:31], v[196:199], v[136:139], v[16:31]\n"
"ds_read_b64_tr_b16 v[188:189], v231 offset:53504\n"
"ds_read_b64_tr_b16 v[190:191], v231 offset:54528\n"
"v_mfma_f32_32x32x16_bf16 v[80:95], v[196:199], v[140:143], v[80:95]\n"
"s_mov_b32 s82, 0xa8000\n"
"s_add_u32 m0, s78, 0x8000\n"
"s_nop 0\n"
"buffer_load_dwordx4 v220, s[44:47], s82 offen lds\n"
"v_mfma_f32_32x32x16_bf16 v[32:47], v[200:203], v[136:139], v[32:47]\n"
"v_mfma_f32_32x32x16_bf16 v[96:111], v[200:203], v[140:143], v[96:111]\n"
"buffer_load_dwordx4 v221, s[44:47], s82 offen offset:1024 lds\n"
"v_mfma_f32_32x32x16_bf16 v[48:63], v[204:207], v[136:139], v[48:63]\n"
"s_waitcnt lgkmcnt(0)\n"
"s_waitcnt vmcnt(6)\n"
"s_mov_b64 exec, 1\n"
"ds_add_u32 v248, v249\n"
"s_mov_b64 exec, -1\n"
"v_mfma_f32_32x32x16_bf16 v[112:127], v[204:207], v[140:143], v[112:127]\n"
"buffer_load_dwordx4 v222, s[44:47], s82 offen offset:2048 lds\n"
"v_mfma_f32_32x32x16_bf16 v[0:15], v[160:163], v[144:147], v[0:15]\n"
"ds_read_b32 v250, v248\n"
"v_mfma_f32_32x32x16_bf16 v[64:79], v[160:163], v[148:151], v[64:79]\n"
"buffer_load_dwordx4 v223, s[44:47], s82 offen offset:3072 lds\n"
"v_mfma_f32_32x32x16_bf16 v[16:31], v[164:167], v[144:147], v[16:31]\n"
"v_mfma_f32_32x32x16_bf16 v[80:95], v[164:167], v[148:151], v[80:95]\n"
"s_waitcnt lgkmcnt(0)\n"
"v_cmp_gt_u32 vcc, 16, v250\n"
"s_cbranch_vccnz .Lslow_c4\n"
".Lret_c4:\n"
"ds_read_b64_tr_b16 v[192:193], v230\n"
"ds_read_b64_tr_b16 v[194:195], v230 offset:1024\n"
"s_mov_b32 s82, 0x180\n"
"s_add_u32 m0, s79, 0x4000\n"
"v_mfma_f32_32x32x16_bf16 v[32:47], v[168:171], v[144:147], v[32:47]\n"
"ds_read_b64_tr_b16 v[196:197], v231\n"
"ds_read_b64_tr_b16 v[198:199], v231 offset:1024\n"
"buffer_load_dwordx4 v216, s[40:43], s82 offen lds\n"
"v_mfma_f32_32x32x16_bf16 v[96:111], v[168:171], v[148:151], v[96:111]\n"
"ds_read_b64_tr_b16 v[200:201], v230 offset:256\n"
"ds_read_b64_tr_b16 v[202:203], v230 offset:1280\n"
"buffer_load_dwordx4 v217, s[40:43], s82 offen offset:1024 lds\n"
"v_mfma_f32_32x32x16_bf16 v[48:63], v[172:175], v[144:147], v[48:63]\n"
"ds_read_b64_tr_b16 v[204:205], v231 offset:256\n"
"ds_read_b64_tr_b16 v[206:207], v231 offset:1280\n"
"v_mfma_f32_32x32x16_bf16 v[112:127], v[172:175], v[148:151], v[112:127]\n"
"ds_read_b64_tr_b16 v[160:161], v230 offset:4096\n"
"ds_read_b64_tr_b16 v[162:163], v230 offset:5120\n"
"buffer_load_dwordx4 v218, s[40:43], s82 offen offset:2048 lds\n"
"v_mfma_f32_32x32x16_bf16 v[0:15], v[176:179], v[152:155], v[0:15]\n"
"ds_read_b64_tr_b16 v[164:165], v231 offset:4096\n"
"ds_read_b64_tr_b16 v[166:167], v231 offset:5120\n"
"v_mfma_f32_32x32x16_bf16 v[64:79], v[176:179], v[156:159], v[64:79]\n"
"ds_read_b64_tr_b16 v[168:169], v230 offset:4352\n"
"ds_read_b64_tr_b16 v[170:171], v230 offset:5376\n"
"buffer_load_dwordx4 v219, s[40:43], s82 offen offset:3072 lds\n"
"v_mfma_f32_32x32x16_bf16 v[16:31], v[180:183], v[152:155], v[16:31]\n"
"ds_read_b64_tr_b16 v[172:173], v231 offset:4352\n"
"ds_read_b64_tr_b16 v[174:175], v231 offset:5376\n"
"ds_read_b128 v[128:131], v226\n"
"ds_read_b128 v[132:135], v226 offset:4096\n"
"v_mfma_f32_32x32x16_bf16 v[80:95], v[180:183], v[156:159], v[80:95]\n"
"ds_read_b128 v[136:139], v227\n"
"ds_read_b128 v[140:143], v227 offset:4096\n"
"s_mov_b32 s82, 0xc4000\n"
"s_add_u32 m0, s78, 0xc000\n"
"s_nop 0\n"
"buffer_load_dwordx4 v220, s[44:47], s82 offen lds\n"
"v_mfma_f32_32x32x16_bf16 v[32:47], v[184:187], v[152:155], v[32:47]\n"
"ds_read_b128 v[144:147], v228\n"
"ds_read_b128 v[148:151], v228 offset:4096\n"
"v_mfma_f32_32x32x16_bf16 v[96:111], v[184:187], v[156:159], v[96:111]\n"
"buffer_load_dwordx4 v221, s[44:47], s82 offen offset:1024 lds\n"
"v_mfma_f32_32x32x16_bf16 v[48:63], v[188:191], v[152:155], v[48:63]\n"
"s_waitcnt lgkmcnt(4)\n"
"s_waitcnt vmcnt(10)\n"
"s_mov_b64 exec, 1\n"
"ds_add_u32 v248, v249\n"
"s_mov_b64 exec, -1\n"
"ds_read_b128 v[152:155], v229\n"
"v_mfma_f32_32x32x16_bf16 v[112:127], v[188:191], v[156:159], v[112:127]\n"
"ds_read_b128 v[156:159], v229 offset:4096\n"
"buffer_load_dwordx4 v222, s[44:47], s82 offen offset:2048 lds\n"
"v_mfma_f32_32x32x16_bf16 v[0:15], v[192:195], v[128:131], v[0:15]\n"
"ds_read_b32 v250, v248\n"
"v_mfma_f32_32x32x16_bf16 v[64:79], v[192:195], v[132:135], v[64:79]\n"
"buffer_load_dwordx4 v223, s[44:47], s82 offen offset:3072 lds\n"
"v_mfma_f32_32x32x16_bf16 v[16:31], v[196:199], v[128:131], v[16:31]\n"
"v_mfma_f32_32x32x16_bf16 v[80:95], v[196:199], v[132:135], v[80:95]\n"
"s_waitcnt lgkmcnt(0)\n"
"v_cmp_gt_u32 vcc, 20, v250\n"
"s_cbranch_vccnz .Lslow_c5\n"
".Lret_c5:\n"
"ds_read_b64_tr_b16 v[176:177], v230 offset:16384\n"
"ds_read_b64_tr_b16 v[178:179], v230 offset:17408\n"
"s_mov_b32 s82, 0xe0000\n"
"s_add_u32 m0, s78, 0\n"
"v_mfma_f32_32x32x16_bf16 v[32:47], v[200:203], v[128:131], v[32:47]\n"
"ds_read_b64_tr_b16 v[180:181], v231 offset:16384\n"
"ds_read_b64_tr_b16 v[182:183], v231 offset:17408\n"
"buffer_load_dwordx4 v220, s[44:47], s82 offen lds\n"
"v_mfma_f32_32x32x16_bf16 v[96:111], v[200:203], v[132:135], v[96:111]\n"
"ds_read_b64_tr_b16 v[184:185], v230 offset:16640\n"
"ds_read_b64_tr_b16 v[186:187], v230 offset:17664\n"
"buffer_load_dwordx4 v221, s[44:47], s82 offen offset:1024 lds\n"
"v_mfma_f32_32x32x16_bf16 v[48:63], v[204:207], v[128:131], v[48:63]\n"
"ds_read_b64_tr_b16 v[188:189], v231 offset:16640\n"
"ds_read_b64_tr_b16 v[190:191], v231 offset:17664\n"
"v_mfma_f32_32x32x16_bf16 v[112:127], v[204:207], v[132:135], v[112:127]\n"
"ds_read_b64_tr_b16 v[192:193], v230 offset:20480\n"
"ds_read_b64_tr_b16 v[194:195], v230 offset:21504\n"
"buffer_load_dwordx4 v222, s[44:47], s82 offen offset:2048 lds\n"
"v_mfma_f32_32x32x16_bf16 v[0:15], v[160:163], v[136:139], v[0:15]\n"
"ds_read_b64_tr_b16 v[196:197], v231 offset:20480\n"
"ds_read_b64_tr_b16 v[198:199], v231 offset:21504\n"
"v_mfma_f32_32x32x16_bf16 v[64:79], v[160:163], v[140:143], v[64:79]\n"
"ds_read_b64_tr_b16 v[200:201], v230 offset:20736\n"
"ds_read_b64_tr_b16 v[202:203], v230 offset:21760\n"
"buffer_load_dwordx4 v223, s[44:47], s82 offen offset:3072 lds\n"
"v_mfma_f32_32x32x16_bf16 v[16:31], v[164:167], v[136:139], v[16:31]\n"
"ds_read_b64_tr_b16 v[204:205], v231 offset:20736\n"
"ds_read_b64_tr_b16 v[206:207], v231 offset:21760\n"
"v_mfma_f32_32x32x16_bf16 v[80:95], v[164:167], v[140:143], v[80:95]\n"
"v_mfma_f32_32x32x16_bf16 v[32:47], v[168:171], v[136:139], v[32:47]\n"
"v_mfma_f32_32x32x16_bf16 v[96:111], v[168:171], v[140:143], v[96:111]\n"
"v_mfma_f32_32x32x16_bf16 v[48:63], v[172:175], v[136:139], v[48:63]\n"
"s_waitcnt lgkmcnt(0)\n"
"s_waitcnt vmcnt(8)\n"
"s_mov_b64 exec, 1\n"
"ds_add_u32 v248, v249\n"
"s_mov_b64 exec, -1\n"
"v_mfma_f32_32x32x16_bf16 v[112:127], v[172:175], v[140:143], v[112:127]\n"
"v_mfma_f32_32x32x16_bf16 v[0:15], v[176:179], v[144:147], v[0:15]\n"
"ds_read_b32 v250, v248\n"
"v_mfma_f32_32x32x16_bf16 v[64:79], v[176:179], v[148:151], v[64:79]\n"
"v_mfma_f32_32x32x16_bf16 v[16:31], v[180:183], v[144:147], v[16:31]\n"
"v_mfma_f32_32x32x16_bf16 v[80:95], v[180:183], v[148:151], v[80:95]\n"
"s_waitcnt lgkmcnt(0)\n"
"v_cmp_gt_u32 vcc, 24, v250\n"
"s_cbranch_vccnz .Lslow_c6\n"
".Lret_c6:\n"
"ds_read_b64_tr_b16 v[160:161], v230 offset:32768\n"
"ds_read_b64_tr_b16 v[162:163], v230 offset:33792\n"
"s_mov_b32 s82, 0x200\n"
"s_add_u32 m0, s79, 0\n"
"v_mfma_f32_32x32x16_bf16 v[32:47], v[184:187], v[144:147], v[32:47]\n"
"ds_read_b64_tr_b16 v[164:165], v231 offset:32768\n"
"ds_read_b64_tr_b16 v[166:167], v231 offset:33792\n"
"buffer_load_dwordx4 v216, s[40:43], s82 offen lds\n"
"v_mfma_f32_32x32x16_bf16 v[96:111], v[184:187], v[148:151], v[96:111]\n"
"ds_read_b64_tr_b16 v[168:169], v230 offset:33024\n"
"ds_read_b64_tr_b16 v[170:171], v230 offset:34048\n"
"buffer_load_dwordx4 v217, s[40:43], s82 offen offset:1024 lds\n"
"v_mfma_f32_32x32x16_bf16 v[48:63], v[188:191], v[144:147], v[48:63]\n"
"ds_read_b64_tr_b16 v[172:173], v231 offset:33024\n"
"ds_read_b64_tr_b16 v[174:175], v231 offset:34048\n"
"v_mfma_f32_32x32x16_bf16 v[112:127], v[188:191], v[148:151], v[112:127]\n"
"ds_read_b64_tr_b16 v[176:177], v230 offset:36864\n"
"ds_read_b64_tr_b16 v[178:179], v230 offset:37888\n"
"buffer_load_dwordx4 v218, s[40:43], s82 offen offset:2048 lds\n"
"v_mfma_f32_32x32x16_bf16 v[0:15], v[192:195], v[152:155], v[0:15]\n"
"ds_read_b64_tr_b16 v[180:181], v231 offset:36864\n"
"ds_read_b64_tr_b16 v[182:183], v231 offset:37888\n"
"v_mfma_f32_32x32x16_bf16 v[64:79], v[192:195], v[156:159], v[64:79]\n"
"ds_read_b64_tr_b16 v[184:185], v230 offset:37120\n"
"ds_read_b64_tr_b16 v[186:187], v230 offset:38144\n"
"buffer_load_dwordx4 v219, s[40:43], s82 offen offset:3072 lds\n"
"v_mfma_f32_32x32x16_bf16 v[16:31], v[196:199], v[152:155], v[16:31]\n"
"ds_read_b64_tr_b16 v[188:189], v231 offset:37120\n"
"ds_read_b64_tr_b16 v[190:191], v231 offset:38144\n"
"ds_read_b128 v[128:131], v226 offset:16384\n"
"ds_read_b128 v[132:135], v226 offset:20480\n"
"v_mfma_f32_32x32x16_bf16 v[80:95], v[196:199], v[156:159], v[80:95]\n"
"ds_read_b128 v[136:139], v227 offset:16384\n"
"ds_read_b128 v[140:143], v227 offset:20480\n"
"s_mov_b32 s82, 0xfc000\n"
"s_add_u32 m0, s78, 0x4000\n"
"s_nop 0\n"
"buffer_load_dwordx4 v220, s[44:47], s82 offen lds\n"
"v_mfma_f32_32x32x16_bf16 v[32:47], v[200:203], v[152:155], v[32:47]\n"
"ds_read_b128 v[144:147], v228 offset:16384\n"
"ds_read_b128 v[148:151], v228 offset:20480\n"
"v_mfma_f32_32x32x16_bf16 v[96:111], v[200:203], v[156:159], v[96:111]\n"
"buffer_load_dwordx4 v221, s[44:47], s82 offen offset:1024 lds\n"
"v_mfma_f32_32x32x16_bf16 v[48:63], v[204:207], v[152:155], v[48:63]\n"
"s_waitcnt lgkmcnt(4)\n"
"s_waitcnt vmcnt(10)\n"
"s_mov_b64 exec, 1\n"
"ds_add_u32 v248, v249\n"
"s_mov_b64 exec, -1\n"
"ds_read_b128 v[152:155], v229 offset:16384\n"
"v_mfma_f32_32x32x16_bf16 v[112:127], v[204:207], v[156:159], v[112:127]\n"
"ds_read_b128 v[156:159], v229 offset:20480\n"
"buffer_load_dwordx4 v222, s[44:47], s82 offen offset:2048 lds\n"
"v_mfma_f32_32x32x16_bf16 v[0:15], v[160:163], v[128:131], v[0:15]\n"
"ds_read_b32 v250, v248\n"
"v_mfma_f32_32x32x16_bf16 v[64:79], v[160:163], v[132:135], v[64:79]\n"
"buffer_load_dwordx4 v223, s[44:47], s82 offen offset:3072 lds\n"
"v_mfma_f32_32x32x16_bf16 v[16:31], v[164:167], v[128:131], v[16:31]\n"
"v_mfma_f32_32x32x16_bf16 v[80:95], v[164:167], v[132:135], v[80:95]\n"
"s_waitcnt lgkmcnt(0)\n"
"v_cmp_gt_u32 vcc, 28, v250\n"
"s_cbranch_vccnz .Lslow_c7\n"
".Lret_c7:\n"
"ds_read_b64_tr_b16 v[192:193], v230 offset:49152\n"
"ds_read_b64_tr_b16 v[194:195], v230 offset:50176\n"
"s_mov_b32 s82, 0x118000\n"
"s_add_u32 m0, s78, 0x8000\n"
"v_mfma_f32_32x32x16_bf16 v[32:47], v[168:171], v[128:131], v[32:47]\n"
"ds_read_b64_tr_b16 v[196:197], v231 offset:49152\n"
"ds_read_b64_tr_b16 v[198:199], v231 offset:50176\n"
"buffer_load_dwordx4 v220, s[44:47], s82 offen lds\n"
"v_mfma_f32_32x32x16_bf16 v[96:111], v[168:171], v[132:135], v[96:111]\n"
"ds_read_b64_tr_b16 v[200:201], v230 offset:49408\n"
"ds_read_b64_tr_b16 v[202:203], v230 offset:50432\n"
"buffer_load_dwordx4 v221, s[44:47], s82 offen offset:1024 lds\n"
"v_mfma_f32_32x32x16_bf16 v[48:63], v[172:175], v[128:131], v[48:63]\n"
"ds_read_b64_tr_b16 v[204:205], v231 offset:49408\n"
"ds_read_b64_tr_b16 v[206:207], v231 offset:50432\n"
"v_mfma_f32_32x32x16_bf16 v[112:127], v[172:175], v[132:135], v[112:127]\n"
"ds_read_b64_tr_b16 v[160:161], v230 offset:53248\n"
"ds_read_b64_tr_b16 v[162:163], v230 offset:54272\n"
"buffer_load_dwordx4 v222, s[44:47], s82 offen offset:2048 lds\n"
"v_mfma_f32_32x32x16_bf16 v[0:15], v[176:179], v[136:139], v[0:15]\n"
"ds_read_b64_tr_b16 v[164:165], v231 offset:53248\n"
"ds_read_b64_tr_b16 v[166:167], v231 offset:54272\n"
"v_mfma_f32_32x32x16_bf16 v[64:79], v[176:179], v[140:143], v[64:79]\n"
"ds_read_b64_tr_b16 v[168:169], v230 offset:53504\n"
"ds_read_b64_tr_b16 v[170:171], v230 offset:54528\n"
"buffer_load_dwordx4 v223, s[44:47], s82 offen offset:3072 lds\n"
"v_mfma_f32_32x32x16_bf16 v[16:31], v[180:183], v[136:139], v[16:31]\n"
"ds_read_b64_tr_b16 v[172:173], v231 offset:53504\n"
"ds_read_b64_tr_b16 v[174:175], v231 offset:54528\n"
"v_mfma_f32_32x32x16_bf16 v[80:95], v[180:183], v[140:143], v[80:95]\n"
"v_mfma_f32_32x32x16_bf16 v[32:47], v[184:187], v[136:139], v[32:47]\n"
"v_mfma_f32_32x32x16_bf16 v[96:111], v[184:187], v[140:143], v[96:111]\n"
"v_mfma_f32_32x32x16_bf16 v[48:63], v[188:191], v[136:139], v[48:63]\n"
"s_waitcnt lgkmcnt(0)\n"
"s_waitcnt vmcnt(8)\n"
"s_mov_b64 exec, 1\n"
"ds_add_u32 v248, v249\n"
"s_mov_b64 exec, -1\n"
"v_mfma_f32_32x32x16_bf16 v[112:127], v[188:191], v[140:143], v[112:127]\n"
"v_mfma_f32_32x32x16_bf16 v[0:15], v[192:195], v[144:147], v[0:15]\n"
"ds_read_b32 v250, v248\n"
"v_mfma_f32_32x32x16_bf16 v[64:79], v[192:195], v[148:151], v[64:79]\n"
"v_mfma_f32_32x32x16_bf16 v[16:31], v[196:199], v[144:147], v[16:31]\n"
"v_mfma_f32_32x32x16_bf16 v[80:95], v[196:199], v[148:151], v[80:95]\n"
"s_waitcnt lgkmcnt(0)\n"
"v_cmp_gt_u32 vcc, 32, v250\n"
"s_cbranch_vccnz .Lslow_c8\n"
".Lret_c8:\n"
"ds_read_b64_tr_b16 v[176:177], v230\n"
"ds_read_b64_tr_b16 v[178:179], v230 offset:1024\n"
"s_mov_b32 s82, 0x280\n"
"s_add_u32 m0, s79, 0x4000\n"
"v_mfma_f32_32x32x16_bf16 v[32:47], v[200:203], v[144:147], v[32:47]\n"
"ds_read_b64_tr_b16 v[180:181], v231\n"
"ds_read_b64_tr_b16 v[182:183], v231 offset:1024\n"
"buffer_load_dwordx4 v216, s[40:43], s82 offen lds\n"
"v_mfma_f32_32x32x16_bf16 v[96:111], v[200:203], v[148:151], v[96:111]\n"
"ds_read_b64_tr_b16 v[184:185], v230 offset:256\n"
"ds_read_b64_tr_b16 v[186:187], v230 offset:1280\n"
"buffer_load_dwordx4 v217, s[40:43], s82 offen offset:1024 lds\n"
"v_mfma_f32_32x32x16_bf16 v[48:63], v[204:207], v[144:147], v[48:63]\n"
"ds_read_b64_tr_b16 v[188:189], v231 offset:256\n"
"ds_read_b64_tr_b16 v[190:191], v231 offset:1280\n"
"v_mfma_f32_32x32x16_bf16 v[112:127], v[204:207], v[148:151], v[112:127]\n"
"ds_read_b64_tr_b16 v[192:193], v230 offset:4096\n"
"ds_read_b64_tr_b16 v[194:195], v230 offset:5120\n"
"buffer_load_dwordx4 v218, s[40:43], s82 offen offset:2048 lds\n"
"v_mfma_f32_32x32x16_bf16 v[0:15], v[160:163], v[152:155], v[0:15]\n"
"ds_read_b64_tr_b16 v[196:197], v231 offset:4096\n"
"ds_read_b64_tr_b16 v[198:199], v231 offset:5120\n"
"v_mfma_f32_32x32x16_bf16 v[64:79], v[160:163], v[156:159], v[64:79]\n"
"ds_read_b64_tr_b16 v[200:201], v230 offset:4352\n"
"ds_read_b64_tr_b16 v[202:203], v230 offset:5376\n"
"buffer_load_dwordx4 v219, s[40:43], s82 offen offset:3072 lds\n"
"v_mfma_f32_32x32x16_bf16 v[16:31], v[164:167], v[152:155], v[16:31]\n"
"ds_read_b64_tr_b16 v[204:205], v231 offset:4352\n"
"ds_read_b64_tr_b16 v[206:207], v231 offset:5376\n"
"ds_read_b128 v[128:131], v226\n"
"ds_read_b128 v[132:135], v226 offset:4096\n"
"v_mfma_f32_32x32x16_bf16 v[80:95], v[164:167], v[156:159], v[80:95]\n"
"ds_read_b128 v[136:139], v227\n"
"ds_read_b128 v[140:143], v227 offset:4096\n"
"s_mov_b32 s82, 0x134000\n"
"s_add_u32 m0, s78, 0xc000\n"
"s_nop 0\n"
"buffer_load_dwordx4 v220, s[44:47], s82 offen lds\n"
"v_mfma_f32_32x32x16_bf16 v[32:47], v[168:171], v[152:155], v[32:47]\n"
"ds_read_b128 v[144:147], v228\n"
"ds_read_b128 v[148:151], v228 offset:4096\n"
"v_mfma_f32_32x32x16_bf16 v[96:111], v[168:171], v[156:159], v[96:111]\n"
"buffer_load_dwordx4 v221, s[44:47], s82 offen offset:1024 lds\n"
"v_mfma_f32_32x32x16_bf16 v[48:63], v[172:175], v[152:155], v[48:63]\n"
"s_waitcnt lgkmcnt(4)\n"
"s_waitcnt vmcnt(10)\n"
"s_mov_b64 exec, 1\n"
"ds_add_u32 v248, v249\n"
"s_mov_b64 exec, -1\n"
"ds_read_b128 v[152:155], v229\n"
"v_mfma_f32_32x32x16_bf16 v[112:127], v[172:175], v[156:159], v[112:127]\n"
"ds_read_b128 v[156:159], v229 offset:4096\n"
"buffer_load_dwordx4 v222, s[44:47], s82 offen offset:2048 lds\n"
"v_mfma_f32_32x32x16_bf16 v[0:15], v[176:179], v[128:131], v[0:15]\n"
"ds_read_b32 v250, v248\n"
"v_mfma_f32_32x32x16_bf16 v[64:79], v[176:179], v[132:135], v[64:79]\n"
"buffer_load_dwordx4 v223, s[44:47], s82 offen offset:3072 lds\n"
"v_mfma_f32_32x32x16_bf16 v[16:31], v[180:183], v[128:131], v[16:31]\n"
"v_mfma_f32_32x32x16_bf16 v[80:95], v[180:183], v[132:135], v[80:95]\n"
"s_waitcnt lgkmcnt(0)\n"
"v_cmp_gt_u32 vcc, 36, v250\n"
"s_cbranch_vccnz .Lslow_c9\n"
".Lret_c9:\n"
"ds_read_b64_tr_b16 v[160:161], v230 offset:16384\n"
"ds_read_b64_tr_b16 v[162:163], v230 offset:17408\n"
"s_mov_b32 s82, 0x150000\n"
"s_add_u32 m0, s78, 0\n"
"v_mfma_f32_32x32x16_bf16 v[32:47], v[184:187], v[128:131], v[32:47]\n"
"ds_read_b64_tr_b16 v[164:165], v231 offset:16384\n"
"ds_read_b64_tr_b16 v[166:167], v231 offset:17408\n"
"buffer_load_dwordx4 v220, s[44:47], s82 offen lds\n"
"v_mfma_f32_32x32x16_bf16 v[96:111], v[184:187], v[132:135], v[96:111]\n"
"ds_read_b64_tr_b16 v[168:169], v230 offset:16640\n"
"ds_read_b64_tr_b16 v[170:171], v230 offset:17664\n"
"buffer_load_dwordx4 v221, s[44:47], s82 offen offset:1024 lds\n"
"v_mfma_f32_32x32x16_bf16 v[48:63], v[188:191], v[128:131], v[48:63]\n"
"ds_read_b64_tr_b16 v[172:173], v231 offset:16640\n"
"ds_read_b64_tr_b16 v[174:175], v231 offset:17664\n"
"v_mfma_f32_32x32x16_bf16 v[112:127], v[188:191], v[132:135], v[112:127]\n"
"ds_read_b64_tr_b16 v[176:177], v230 offset:20480\n"
"ds_read_b64_tr_b16 v[178:179], v230 offset:21504\n"
"buffer_load_dwordx4 v222, s[44:47], s82 offen offset:2048 lds\n"
"v_mfma_f32_32x32x16_bf16 v[0:15], v[192:195], v[136:139], v[0:15]\n"
"ds_read_b64_tr_b16 v[180:181], v231 offset:20480\n"
"ds_read_b64_tr_b16 v[182:183], v231 offset:21504\n"
"v_mfma_f32_32x32x16_bf16 v[64:79], v[192:195], v[140:143], v[64:79]\n"
"ds_read_b64_tr_b16 v[184:185], v230 offset:20736\n"
"ds_read_b64_tr_b16 v[186:187], v230 offset:21760\n"
"buffer_load_dwordx4 v223, s[44:47], s82 offen offset:3072 lds\n"
"v_mfma_f32_32x32x16_bf16 v[16:31], v[196:199], v[136:139], v[16:31]\n"
"ds_read_b64_tr_b16 v[188:189], v231 offset:20736\n"
"ds_read_b64_tr_b16 v[190:191], v231 offset:21760\n"
"v_mfma_f32_32x32x16_bf16 v[80:95], v[196:199], v[140:143], v[80:95]\n"
"v_mfma_f32_32x32x16_bf16 v[32:47], v[200:203], v[136:139], v[32:47]\n"
"v_mfma_f32_32x32x16_bf16 v[96:111], v[200:203], v[140:143], v[96:111]\n"
"v_mfma_f32_32x32x16_bf16 v[48:63], v[204:207], v[136:139], v[48:63]\n"
"s_waitcnt lgkmcnt(0)\n"
"s_waitcnt vmcnt(8)\n"
"s_mov_b64 exec, 1\n"
"ds_add_u32 v248, v249\n"
"s_mov_b64 exec, -1\n"
"v_mfma_f32_32x32x16_bf16 v[112:127], v[204:207], v[140:143], v[112:127]\n"
"v_mfma_f32_32x32x16_bf16 v[0:15], v[160:163], v[144:147], v[0:15]\n"
"ds_read_b32 v250, v248\n"
"v_mfma_f32_32x32x16_bf16 v[64:79], v[160:163], v[148:151], v[64:79]\n"
"v_mfma_f32_32x32x16_bf16 v[16:31], v[164:167], v[144:147], v[16:31]\n"
"v_mfma_f32_32x32x16_bf16 v[80:95], v[164:167], v[148:151], v[80:95]\n"
"s_waitcnt lgkmcnt(0)\n"
"v_cmp_gt_u32 vcc, 40, v250\n"
"s_cbranch_vccnz .Lslow_c10\n"
".Lret_c10:\n"
"ds_read_b64_tr_b16 v[192:193], v230 offset:32768\n"
"ds_read_b64_tr_b16 v[194:195], v230 offset:33792\n"
"s_mov_b32 s82, 0x300\n"
"s_add_u32 m0, s79, 0\n"
"v_mfma_f32_32x32x16_bf16 v[32:47], v[168:171], v[144:147], v[32:47]\n"
"ds_read_b64_tr_b16 v[196:197], v231 offset:32768\n"
"ds_read_b64_tr_b16 v[198:199], v231 offset:33792\n"
"buffer_load_dwordx4 v216, s[40:43], s82 offen lds\n"
"v_mfma_f32_32x32x16_bf16 v[96:111], v[168:171], v[148:151], v[96:111]\n"
"ds_read_b64_tr_b16 v[200:201], v230 offset:33024\n"
"ds_read_b64_tr_b16 v[202:203], v230 offset:34048\n"
"buffer_load_dwordx4 v217, s[40:43], s82 offen offset:1024 lds\n"
"v_mfma_f32_32x32x16_bf16 v[48:63], v[172:175], v[144:147], v[48:63]\n"
"ds_read_b64_tr_b16 v[204:205], v231 offset:33024\n"
"ds_read_b64_tr_b16 v[206:207], v231 offset:34048\n"
"v_mfma_f32_32x32x16_bf16 v[112:127], v[172:175], v[148:151], v[112:127]\n"
"ds_read_b64_tr_b16 v[160:161], v230 offset:36864\n"
"ds_read_b64_tr_b16 v[162:163], v230 offset:37888\n"
"buffer_load_dwordx4 v218, s[40:43], s82 offen offset:2048 lds\n"
"v_mfma_f32_32x32x16_bf16 v[0:15], v[176:179], v[152:155], v[0:15]\n"
"ds_read_b64_tr_b16 v[164:165], v231 offset:36864\n"
"ds_read_b64_tr_b16 v[166:167], v231 offset:37888\n"
"v_mfma_f32_32x32x16_bf16 v[64:79], v[176:179], v[156:159], v[64:79]\n"
"ds_read_b64_tr_b16 v[168:169], v230 offset:37120\n"
"ds_read_b64_tr_b16 v[170:171], v230 offset:38144\n"
"buffer_load_dwordx4 v219, s[40:43], s82 offen offset:3072 lds\n"
"v_mfma_f32_32x32x16_bf16 v[16:31], v[180:183], v[152:155], v[16:31]\n"
"ds_read_b64_tr_b16 v[172:173], v231 offset:37120\n"
"ds_read_b64_tr_b16 v[174:175], v231 offset:38144\n"
"ds_read_b128 v[128:131], v226 offset:16384\n"
"ds_read_b128 v[132:135], v226 offset:20480\n"
"v_mfma_f32_32x32x16_bf16 v[80:95], v[180:183], v[156:159], v[80:95]\n"
"ds_read_b128 v[136:139], v227 offset:16384\n"
"ds_read_b128 v[140:143], v227 offset:20480\n"
"s_mov_b32 s82, 0x16c000\n"
"s_add_u32 m0, s78, 0x4000\n"
"s_nop 0\n"
"buffer_load_dwordx4 v220, s[44:47], s82 offen lds\n"
"v_mfma_f32_32x32x16_bf16 v[32:47], v[184:187], v[152:155], v[32:47]\n"
"ds_read_b128 v[144:147], v228 offset:16384\n"
"ds_read_b128 v[148:151], v228 offset:20480\n"
"v_mfma_f32_32x32x16_bf16 v[96:111], v[184:187], v[156:159], v[96:111]\n"
"buffer_load_dwordx4 v221, s[44:47], s82 offen offset:1024 lds\n"
"v_mfma_f32_32x32x16_bf16 v[48:63], v[188:191], v[152:155], v[48:63]\n"
"s_waitcnt lgkmcnt(4)\n"
"s_waitcnt vmcnt(10)\n"
"s_mov_b64 exec, 1\n"
"ds_add_u32 v248, v249\n"
"s_mov_b64 exec, -1\n"
"ds_read_b128 v[152:155], v229 offset:16384\n"
"v_mfma_f32_32x32x16_bf16 v[112:127], v[188:191], v[156:159], v[112:127]\n"
"ds_read_b128 v[156:159], v229 offset:20480\n"
"buffer_load_dwordx4 v222, s[44:47], s82 offen offset:2048 lds\n"
"v_mfma_f32_32x32x16_bf16 v[0:15], v[192:195], v[128:131], v[0:15]\n"
"ds_read_b32 v250, v248\n"
"v_mfma_f32_32x32x16_bf16 v[64:79], v[192:195], v[132:135], v[64:79]\n"
"buffer_load_dwordx4 v223, s[44:47], s82 offen offset:3072 lds\n"
"v_mfma_f32_32x32x16_bf16 v[16:31], v[196:199], v[128:131], v[16:31]\n"
"v_mfma_f32_32x32x16_bf16 v[80:95], v[196:199], v[132:135], v[80:95]\n"
"s_waitcnt lgkmcnt(0)\n"
"v_cmp_gt_u32 vcc, 44, v250\n"
"s_cbranch_vccnz .Lslow_c11\n"
".Lret_c11:\n"
"ds_read_b64_tr_b16 v[176:177], v230 offset:49152\n"
"ds_read_b64_tr_b16 v[178:179], v230 offset:50176\n"
"v_mfma_f32_32x32x16_bf16 v[32:47], v[200:203], v[128:131], v[32:47]\n"
"ds_read_b64_tr_b16 v[180:181], v231 offset:49152\n"
"ds_read_b64_tr_b16 v[182:183], v231 offset:50176\n"
"v_mfma_f32_32x32x16_bf16 v[96:111], v[200:203], v[132:135], v[96:111]\n"
"ds_read_b64_tr_b16 v[184:185], v230 offset:49408\n"
"ds_read_b64_tr_b16 v[186:187], v230 offset:50432\n"
"buffer_load_dwordx2 v[216:217], v236, s[52:55], 0 offen sc1\n"
"v_mfma_f32_32x32x16_bf16 v[48:63], v[204:207], v[128:131], v[48:63]\n"
"ds_read_b64_tr_b16 v[188:189], v231 offset:49408\n"
"ds_read_b64_tr_b16 v[190:191], v231 offset:50432\n"
"buffer_load_dwordx2 v[218:219], v236, s[52:55], 0 offen offset:256 sc1\n"
"v_mfma_f32_32x32x16_bf16 v[112:127], v[204:207], v[132:135], v[112:127]\n"
"ds_read_b64_tr_b16 v[192:193], v230 offset:53248\n"
"ds_read_b64_tr_b16 v[194:195], v230 offset:54272\n"
"buffer_load_dwordx2 v[220:221], v236, s[52:55], 0 offen offset:512 sc1\n"
"v_mfma_f32_32x32x16_bf16 v[0:15], v[160:163], v[136:139], v[0:15]\n"
"ds_read_b64_tr_b16 v[196:197], v231 offset:53248\n"
"ds_read_b64_tr_b16 v[198:199], v231 offset:54272\n"
"buffer_load_dwordx2 v[222:223], v236, s[52:55], 0 offen offset:768 sc1\n"
"v_mfma_f32_32x32x16_bf16 v[64:79], v[160:163], v[140:143], v[64:79]\n"
"ds_read_b64_tr_b16 v[200:201], v230 offset:53504\n"
"ds_read_b64_tr_b16 v[202:203], v230 offset:54528\n"
"buffer_load_dwordx2 v[208:209], v235, s[52:55], 0 offen sc1\n"
"v_mfma_f32_32x32x16_bf16 v[16:31], v[164:167], v[136:139], v[16:31]\n"
"ds_read_b64_tr_b16 v[204:205], v231 offset:53504\n"
"ds_read_b64_tr_b16 v[206:207], v231 offset:54528\n"
"buffer_load_dwordx2 v[212:213], v235, s[52:55], 0 offen offset:256 sc1\n"
"v_mfma_f32_32x32x16_bf16 v[80:95], v[164:167], v[140:143], v[80:95]\n"
"v_mfma_f32_32x32x16_bf16 v[32:47], v[168:171], v[136:139], v[32:47]\n"
"v_mfma_f32_32x32x16_bf16 v[96:111], v[168:171], v[140:143], v[96:111]\n"
"v_mfma_f32_32x32x16_bf16 v[48:63], v[172:175], v[136:139], v[48:63]\n"
"s_waitcnt lgkmcnt(0)\n"
"s_waitcnt vmcnt(6)\n"
"s_mov_b64 exec, 1\n"
"ds_add_u32 v248, v249\n"
"s_mov_b64 exec, -1\n"
"v_mfma_f32_32x32x16_bf16 v[112:127], v[172:175], v[140:143], v[112:127]\n"
"v_mfma_f32_32x32x16_bf16 v[0:15], v[176:179], v[144:147], v[0:15]\n"
"ds_read_b32 v250, v248\n"
"v_mfma_f32_32x32x16_bf16 v[64:79], v[176:179], v[148:151], v[64:79]\n"
"v_mfma_f32_32x32x16_bf16 v[16:31], v[180:183], v[144:147], v[16:31]\n"
"v_mfma_f32_32x32x16_bf16 v[80:95], v[180:183], v[148:151], v[80:95]\n"
"s_waitcnt lgkmcnt(0)\n"
"v_cmp_gt_u32 vcc, 48, v250\n"
"s_cbranch_vccnz .Lslow_c12\n"
".Lret_c12:\n"
"ds_read_b64_tr_b16 v[160:161], v230\n"
"ds_read_b64_tr_b16 v[162:163], v230 offset:1024\n"
"v_readlane_b32 s86, v241, 0\n"
"s_add_u32 m0, s80, 0x8000\n"
"s_nop 2\n"
"v_mfma_f32_32x32x16_bf16 v[32:47], v[184:187], v[144:147], v[32:47]\n"
"ds_read_b128 v[128:131], v226\n"
"ds_read_b64_tr_b16 v[164:165], v230 offset:4096\n"
"buffer_load_dwordx4 v242, s[48:51], s86 offen lds\n"
"v_mfma_f32_32x32x16_bf16 v[96:111], v[184:187], v[148:151], v[96:111]\n"
"ds_read_b64_tr_b16 v[166:167], v230 offset:5120\n"
"ds_read_b128 v[136:139], v227\n"
"buffer_load_dwordx4 v243, s[48:51], s86 offen offset:1024 lds\n"
"s_waitcnt vmcnt(7)\n"
"v_xor_b32 v251, s60, v217\n"
"s_waitcnt vmcnt(6)\n"
"v_xor_b32 v252, s60, v219\n"
"v_mfma_f32_32x32x16_bf16 v[48:63], v[188:191], v[144:147], v[48:63]\n"
"ds_read_b64_tr_b16 v[168:169], v230 offset:16384\n"
"ds_read_b64_tr_b16 v[170:171], v230 offset:17408\n"
"v_readlane_b32 s87, v241, 1\n"
"s_add_u32 m0, s80, 0x8800\n"
"s_nop 2\n"
"s_waitcnt vmcnt(5)\n"
"v_xor_b32 v253, s60, v221\n"
"v_or3_b32 v251, v251, v252, v253\n"
"v_mfma_f32_32x32x16_bf16 v[112:127], v[188:191], v[148:151], v[112:127]\n"
"ds_read_b128 v[144:147], v228\n"
"ds_read_b64_tr_b16 v[172:173], v230 offset:20480\n"
"buffer_load_dwordx4 v242, s[48:51], s87 offen lds\n"
"s_waitcnt vmcnt(5)\n"
"v_xor_b32 v252, s60, v223\n"
"s_waitcnt vmcnt(4)\n"
"v_xor_b32 v253, s60, v209\n"
"v_mfma_f32_32x32x16_bf16 v[0:15], v[192:195], v[152:155], v[0:15]\n"
"ds_read_b64_tr_b16 v[174:175], v230 offset:21504\n"
"buffer_load_dwordx4 v243, s[48:51], s87 offen offset:1024 lds\n"
"v_or3_b32 v251, v251, v252, v253\n"
"s_waitcnt vmcnt(4)\n"
"v_xor_b32 v252, s60, v213\n"
"v_or_b32 v251, v251, v252\n"
"v_mfma_f32_32x32x16_bf16 v[64:79], v[192:195], v[156:159], v[64:79]\n"
"v_readlane_b32 s86, v241, 2\n"
"s_add_u32 m0, s80, 0xc000\n"
"s_nop 2\n"
"s_nop 0\n"
"buffer_load_dwordx4 v242, s[48:51], s86 offen lds\n"
"v_mfma_f32_32x32x16_bf16 v[16:31], v[196:199], v[152:155], v[16:31]\n"
"buffer_load_dwordx4 v243, s[48:51], s86 offen offset:1024 lds\n"
"v_cmp_ne_u32 vcc, 0, v251\n"
"s_cbranch_vccnz .Lslow_cs\n"
".Lret_cs:\n"
"s_mov_b32 s75, 0x7060302\n"
"v_cmp_gt_u32 s[36:37], 32, v232\n"
"v_mfma_f32_32x32x16_bf16 v[80:95], v[196:199], v[156:159], v[80:95]\n"
"v_mov_b32 v224, 0\n"
"v_mov_b32 v225, 0\n"
"v_and_b32 v251, s39, v216\n"
"v_sub_f32 v251, v216, v251\n"
"v_mfma_f32_32x32x16_bf16 v[32:47], v[200:203], v[152:155], v[32:47]\n"
"v_readlane_b32 s87, v241, 3\n"
"s_add_u32 m0, s80, 0xc800\n"
"s_nop 2\n"
"s_nop 0\n"
"buffer_load_dwordx4 v242, s[48:51], s87 offen lds\n"
"v_and_b32 v252, s39, v251\n"
"v_mfma_f32_32x32x16_bf16 v[96:111], v[200:203], v[156:159], v[96:111]\n"
"buffer_load_dwordx4 v243, s[48:51], s87 offen offset:1024 lds\n"
"v_perm_b32 v216, v251, v216, s75\n"
"v_sub_f32 v251, v251, v252\n"
"v_lshrrev_b32 v217, 16, v251\n"
"v_and_b32 v251, s39, v218\n"
"v_mfma_f32_32x32x16_bf16 v[48:63], v[204:207], v[152:155], v[48:63]\n"
"ds_read_b128 v[152:155], v229\n"
"ds_read_b128 v[132:135], v226 offset:4096\n"
"v_sub_f32 v251, v218, v251\n"
"v_and_b32 v252, s39, v251\n"
"v_perm_b32 v218, v251, v218, s75\n"
"v_sub_f32 v251, v251, v252\n"
"v_mfma_f32_32x32x16_bf16 v[112:127], v[204:207], v[156:159], v[112:127]\n"
"ds_read_b128 v[140:143], v227 offset:4096\n"
"ds_read_b128 v[148:151], v228 offset:4096\n"
"v_readlane_b32 s86, v241, 4\n"
"s_add_u32 m0, s80, 0x14000\n"
"s_nop 2\n"
"v_lshrrev_b32 v219, 16, v251\n"
"v_and_b32 v251, s39, v220\n"
"s_waitcnt lgkmcnt(2)\n"
"v_mfma_f32_32x32x16_bf16 v[0:15], v[160:163], v[128:131], v[0:15]\n"
"ds_read_b128 v[156:159], v229 offset:4096\n"
"ds_read_b64_tr_b16 v[176:177], v231\n"
"buffer_load_dwordx4 v242, s[48:51], s86 offen lds\n"
"v_sub_f32 v251, v220, v251\n"
"v_and_b32 v252, s39, v251\n"
"v_mfma_f32_32x32x16_bf16 v[0:15], v[164:167], v[136:139], v[0:15]\n"
"ds_read_b64_tr_b16 v[178:179], v231 offset:1024\n"
"ds_read_b64_tr_b16 v[180:181], v231 offset:4096\n"
"buffer_load_dwordx4 v243, s[48:51], s86 offen offset:1024 lds\n"
"v_perm_b32 v220, v251, v220, s75\n"
"v_sub_f32 v251, v251, v252\n"
"v_mfma_f32_32x32x16_bf16 v[0:15], v[168:171], v[144:147], v[0:15]\n"
"ds_read_b64_tr_b16 v[182:183], v231 offset:5120\n"
"ds_read_b64_tr_b16 v[184:185], v231 offset:16384\n"
"v_readlane_b32 s87, v241, 5\n"
"s_add_u32 m0, s80, 0x14800\n"
"s_nop 2\n"
"v_lshrrev_b32 v221, 16, v251\n"
"v_and_b32 v251, s39, v222\n"
"v_mfma_f32_32x32x16_bf16 v[0:15], v[172:175], v[152:155], v[0:15]\n"
"v_sub_f32 v251, v222, v251\n"
"v_and_b32 v252, s39, v251\n"
"v_perm_b32 v222, v251, v222, s75\n"
"v_sub_f32 v251, v251, v252\n"
"v_lshrrev_b32 v223, 16, v251\n"
"v_cndmask_b32 v208, 0, v208, s[36:37]\n"
"v_and_b32 v209, 0xffff, v208\n"
"v_cndmask_b32 v212, 0, v212, s[36:37]\n"
"v_and_b32 v213, 0xffff, v212\n"
"v_mfma_f32_32x32x16_bf16 v[0:15], v[216:219], v[208:211], v[0:15]\n"
"ds_read_b64_tr_b16 v[186:187], v231 offset:17408\n"
"ds_read_b64_tr_b16 v[188:189], v231 offset:20480\n"
"buffer_load_dwordx4 v242, s[48:51], s87 offen lds\n"
"v_mov_b32 v251, 0x27e74\n"
"v_mfma_f32_32x32x16_bf16 v[64:79], v[160:163], v[132:135], v[64:79]\n"
"ds_read_b64_tr_b16 v[190:191], v231 offset:21504\n"
"ds_read_b64_tr_b16 v[192:193], v230 offset:256\n"
"buffer_load_dwordx4 v243, s[48:51], s87 offen offset:1024 lds\n"
"s_waitcnt lgkmcnt(4)\n"
"v_mfma_f32_32x32x16_bf16 v[64:79], v[164:167], v[140:143], v[64:79]\n"
"ds_read_b64_tr_b16 v[194:195], v230 offset:1280\n"
"ds_read_b64_tr_b16 v[196:197], v230 offset:4352\n"
"v_max_f32 v0, 0, v0\n"
"v_max_f32 v1, 0, v1\n"
"v_max_f32 v2, 0, v2\n"
"v_max_f32 v3, 0, v3\n"
"v_mfma_f32_32x32x16_bf16 v[64:79], v[168:171], v[148:151], v[64:79]\n"
"ds_read_b64_tr_b16 v[198:199], v230 offset:5376\n"
"ds_read_b64_tr_b16 v[200:201], v230 offset:16640\n"
"v_max_f32 v4, 0, v4\n"
"v_max_f32 v5, 0, v5\n"
"v_max_f32 v6, 0, v6\n"
"v_max_f32 v7, 0, v7\n"
"v_mfma_f32_32x32x16_bf16 v[64:79], v[172:175], v[156:159], v[64:79]\n"
"ds_read_b64_tr_b16 v[202:203], v230 offset:17664\n"
"ds_read_b64_tr_b16 v[204:205], v230 offset:20736\n"
"v_mul_f32 v0, v0, v8\n"
"v_mul_f32 v1, v1, v9\n"
"v_mul_f32 v2, v2, v10\n"
"v_mul_f32 v3, v3, v11\n"
"v_mfma_f32_32x32x16_bf16 v[64:79], v[216:219], v[212:215], v[64:79]\n"
"ds_read_b64_tr_b16 v[206:207], v230 offset:21760\n"
"ds_read_b64_tr_b16 v[160:161], v231 offset:256\n"
"v_mul_f32 v4, v4, v12\n"
"v_mul_f32 v5, v5, v13\n"
"v_mul_f32 v6, v6, v14\n"
"v_mul_f32 v7, v7, v15\n"
"v_mfma_f32_32x32x16_bf16 v[16:31], v[176:179], v[128:131], v[16:31]\n"
"ds_read_b64_tr_b16 v[162:163], v231 offset:1280\n"
"ds_read_b64_tr_b16 v[164:165], v231 offset:4352\n"
"v_cvt_pk_bf16_f32 v0, v0, v1\n"
"v_cvt_pk_bf16_f32 v1, v2, v3\n"
"v_cvt_pk_bf16_f32 v2, v4, v5\n"
"v_cvt_pk_bf16_f32 v3, v6, v7\n"
"v_mfma_f32_32x32x16_bf16 v[16:31], v[180:183], v[136:139], v[16:31]\n"
"ds_read_b64_tr_b16 v[166:167], v231 offset:5376\n"
"ds_read_b64_tr_b16 v[168:169], v231 offset:16640\n"
"buffer_store_dwordx4 v[0:3], v246, s[52:55], 0 offen sc1\n"
"s_waitcnt lgkmcnt(4)\n"
"v_mfma_f32_32x32x16_bf16 v[16:31], v[184:187], v[144:147], v[16:31]\n"
"ds_read_b64_tr_b16 v[170:171], v231 offset:17664\n"
"ds_read_b64_tr_b16 v[172:173], v231 offset:20736\n"
"v_max_f32 v64, 0, v64\n"
"v_max_f32 v65, 0, v65\n"
"v_max_f32 v66, 0, v66\n"
"v_max_f32 v67, 0, v67\n"
"v_mfma_f32_32x32x16_bf16 v[16:31], v[188:191], v[152:155], v[16:31]\n"
"ds_read_b64_tr_b16 v[174:175], v231 offset:21760\n"
"v_max_f32 v68, 0, v68\n"
"v_max_f32 v69, 0, v69\n"
"v_max_f32 v70, 0, v70\n"
"v_max_f32 v71, 0, v71\n"
"ds_read_b32 v252, v251\n"
"v_mfma_f32_32x32x16_bf16 v[16:31], v[218:221], v[208:211], v[16:31]\n"
"v_mul_f32 v64, v64, v72\n"
"v_mul_f32 v65, v65, v73\n"
"v_mul_f32 v66, v66, v74\n"
"v_mul_f32 v67, v67, v75\n"
"v_mfma_f32_32x32x16_bf16 v[80:95], v[176:179], v[132:135], v[80:95]\n"
"v_mul_f32 v68, v68, v76\n"
"v_mul_f32 v69, v69, v77\n"
"v_mul_f32 v70, v70, v78\n"
"v_mul_f32 v71, v71, v79\n"
"v_mfma_f32_32x32x16_bf16 v[80:95], v[180:183], v[140:143], v[80:95]\n"
"v_cvt_pk_bf16_f32 v64, v64, v65\n"
"v_cvt_pk_bf16_f32 v65, v66, v67\n"
"v_cvt_pk_bf16_f32 v66, v68, v69\n"
"v_cvt_pk_bf16_f32 v67, v70, v71\n"
"v_mfma_f32_32x32x16_bf16 v[80:95], v[184:187], v[148:151], v[80:95]\n"
"buffer_store_dwordx4 v[64:67], v246, s[52:55], s91 offen sc1\n"
"v_max_f32 v16, 0, v16\n"
"v_max_f32 v17, 0, v17\n"
"v_mfma_f32_32x32x16_bf16 v[80:95], v[188:191], v[156:159], v[80:95]\n"
"v_max_f32 v18, 0, v18\n"
"v_max_f32 v19, 0, v19\n"
"v_max_f32 v20, 0, v20\n"
"v_max_f32 v21, 0, v21\n"
"v_mfma_f32_32x32x16_bf16 v[80:95], v[218:221], v[212:215], v[80:95]\n"
"v_max_f32 v22, 0, v22\n"
"v_max_f32 v23, 0, v23\n"
"v_mul_f32 v16, v16, v24\n"
"v_mul_f32 v17, v17, v25\n"
"v_mfma_f32_32x32x16_bf16 v[32:47], v[192:195], v[128:131], v[32:47]\n"
"v_mul_f32 v18, v18, v26\n"
"v_mul_f32 v19, v19, v27\n"
"v_mul_f32 v20, v20, v28\n"
"v_mul_f32 v21, v21, v29\n"
"v_mfma_f32_32x32x16_bf16 v[32:47], v[196:199], v[136:139], v[32:47]\n"
"v_mul_f32 v22, v22, v30\n"
"v_mul_f32 v23, v23, v31\n"
"v_cvt_pk_bf16_f32 v16, v16, v17\n"
"v_cvt_pk_bf16_f32 v17, v18, v19\n"
"v_mfma_f32_32x32x16_bf16 v[32:47], v[200:203], v[144:147], v[32:47]\n"
"v_cvt_pk_bf16_f32 v18, v20, v21\n"
"v_cvt_pk_bf16_f32 v19, v22, v23\n"
"buffer_store_dwordx4 v[16:19], v246, s[52:55], 0 offen offset:1024 sc1\n"
"v_mfma_f32_32x32x16_bf16 v[32:47], v[204:207], v[152:155], v[32:47]\n"
"v_max_f32 v80, 0, v80\n"
"v_max_f32 v81, 0, v81\n"
"v_max_f32 v82, 0, v82\n"
"v_max_f32 v83, 0, v83\n"
"v_mfma_f32_32x32x16_bf16 v[32:47], v[220:223], v[208:211], v[32:47]\n"
"v_max_f32 v84, 0, v84\n"
"v_max_f32 v85, 0, v85\n"
"v_max_f32 v86, 0, v86\n"
"v_max_f32 v87, 0, v87\n"
"v_mfma_f32_32x32x16_bf16 v[96:111], v[192:195], v[132:135], v[96:111]\n"
"v_mul_f32 v80, v80, v88\n"
"v_mul_f32 v81, v81, v89\n"
"v_mul_f32 v82, v82, v90\n"
"v_mul_f32 v83, v83, v91\n"
"v_mfma_f32_32x32x16_bf16 v[96:111], v[196:199], v[140:143], v[96:111]\n"
"v_mul_f32 v84, v84, v92\n"
"v_mul_f32 v85, v85, v93\n"
"v_mul_f32 v86, v86, v94\n"
"v_mul_f32 v87, v87, v95\n"
"v_mfma_f32_32x32x16_bf16 v[96:111], v[200:203], v[148:151], v[96:111]\n"
"v_cvt_pk_bf16_f32 v80, v80, v81\n"
"v_cvt_pk_bf16_f32 v81, v82, v83\n"
"v_cvt_pk_bf16_f32 v82, v84, v85\n"
"v_cvt_pk_bf16_f32 v83, v86, v87\n"
"v_mfma_f32_32x32x16_bf16 v[96:111], v[204:207], v[156:159], v[96:111]\n"
"buffer_store_dwordx4 v[80:83], v246, s[52:55], s91 offen offset:1024 sc1\n"
"v_max_f32 v32, 0, v32\n"
"v_max_f32 v33, 0, v33\n"
"v_mfma_f32_32x32x16_bf16 v[96:111], v[220:223], v[212:215], v[96:111]\n"
"v_max_f32 v34, 0, v34\n"
"v_max_f32 v35, 0, v35\n"
"v_max_f32 v36, 0, v36\n"
"v_max_f32 v37, 0, v37\n"
"s_waitcnt lgkmcnt(0)\n"
"v_mfma_f32_32x32x16_bf16 v[48:63], v[160:163], v[128:131], v[48:63]\n"
"v_max_f32 v38, 0, v38\n"
"v_max_f32 v39, 0, v39\n"
"v_mul_f32 v32, v32, v40\n"
"v_mul_f32 v33, v33, v41\n"
"v_mfma_f32_32x32x16_bf16 v[48:63], v[164:167], v[136:139], v[48:63]\n"
"v_mul_f32 v34, v34, v42\n"
"v_mul_f32 v35, v35, v43\n"
"v_mul_f32 v36, v36, v44\n"
"v_mul_f32 v37, v37, v45\n"
"v_mfma_f32_32x32x16_bf16 v[48:63], v[168:171], v[144:147], v[48:63]\n"
"v_mul_f32 v38, v38, v46\n"
"v_mul_f32 v39, v39, v47\n"
"v_cvt_pk_bf16_f32 v32, v32, v33\n"
"v_cvt_pk_bf16_f32 v33, v34, v35\n"
"v_mfma_f32_32x32x16_bf16 v[48:63], v[172:175], v[152:155], v[48:63]\n"
"v_cvt_pk_bf16_f32 v34, v36, v37\n"
"v_cvt_pk_bf16_f32 v35, v38, v39\n"
"buffer_store_dwordx4 v[32:35], v246, s[52:55], 0 offen offset:2048 sc1\n"
"v_mfma_f32_32x32x16_bf16 v[48:63], v[222:225], v[208:211], v[48:63]\n"
"s_mov_b64 exec, 1\n"
"ds_add_u32 v248, v249\n"
"s_mov_b64 exec, -1\n"
"v_max_f32 v96, 0, v96\n"
"v_max_f32 v97, 0, v97\n"
"v_mfma_f32_32x32x16_bf16 v[112:127], v[160:163], v[132:135], v[112:127]\n"
"v_max_f32 v98, 0, v98\n"
"v_max_f32 v99, 0, v99\n"
"v_max_f32 v100, 0, v100\n"
"v_max_f32 v101, 0, v101\n"
"v_mfma_f32_32x32x16_bf16 v[112:127], v[164:167], v[140:143], v[112:127]\n"
"v_max_f32 v102, 0, v102\n"
"v_max_f32 v103, 0, v103\n"
"v_mul_f32 v96, v96, v104\n"
"v_mul_f32 v97, v97, v105\n"
"v_mfma_f32_32x32x16_bf16 v[112:127], v[168:171], v[148:151], v[112:127]\n"
"v_mul_f32 v98, v98, v106\n"
"v_mul_f32 v99, v99, v107\n"
"v_mul_f32 v100, v100, v108\n"
"v_mul_f32 v101, v101, v109\n"
"v_mfma_f32_32x32x16_bf16 v[112:127], v[172:175], v[156:159], v[112:127]\n"
"v_mul_f32 v102, v102, v110\n"
"v_mul_f32 v103, v103, v111\n"
"v_cvt_pk_bf16_f32 v96, v96, v97\n"
"v_cvt_pk_bf16_f32 v97, v98, v99\n"
"v_mfma_f32_32x32x16_bf16 v[112:127], v[222:225], v[212:215], v[112:127]\n"
"v_cvt_pk_bf16_f32 v98, v100, v101\n"
"v_cvt_pk_bf16_f32 v99, v102, v103\n"
"buffer_store_dwordx4 v[96:99], v246, s[52:55], s91 offen offset:2048 sc1\n"
"v_max_f32 v48, 0, v48\n"
"v_max_f32 v49, 0, v49\n"
"v_max_f32 v50, 0, v50\n"
"v_max_f32 v51, 0, v51\n"
"v_max_f32 v52, 0, v52\n"
"v_max_f32 v53, 0, v53\n"
"v_max_f32 v54, 0, v54\n"
"v_max_f32 v55, 0, v55\n"
"v_mul_f32 v48, v48, v56\n"
"v_mul_f32 v49, v49, v57\n"
"v_mul_f32 v50, v50, v58\n"
"v_mul_f32 v51, v51, v59\n"
"v_mul_f32 v52, v52, v60\n"
"v_mul_f32 v53, v53, v61\n"
"v_mul_f32 v54, v54, v62\n"
"v_mul_f32 v55, v55, v63\n"
"v_cvt_pk_bf16_f32 v48, v48, v49\n"
"v_cvt_pk_bf16_f32 v49, v50, v51\n"
"v_cvt_pk_bf16_f32 v50, v52, v53\n"
"v_cvt_pk_bf16_f32 v51, v54, v55\n"
"buffer_store_dwordx4 v[48:51], v246, s[52:55], 0 offen offset:3072 sc1\n"
"v_max_f32 v112, 0, v112\n"
"v_max_f32 v113, 0, v113\n"
"v_max_f32 v114, 0, v114\n"
"v_max_f32 v115, 0, v115\n"
"v_max_f32 v116, 0, v116\n"
"v_max_f32 v117, 0, v117\n"
"v_max_f32 v118, 0, v118\n"
"v_max_f32 v119, 0, v119\n"
"v_mul_f32 v112, v112, v120\n"
"v_mul_f32 v113, v113, v121\n"
"v_mul_f32 v114, v114, v122\n"
"v_mul_f32 v115, v115, v123\n"
"v_mul_f32 v116, v116, v124\n"
"v_mul_f32 v117, v117, v125\n"
"v_mul_f32 v118, v118, v126\n"
"v_mul_f32 v119, v119, v127\n"
"v_cvt_pk_bf16_f32 v112, v112, v113\n"
"v_cvt_pk_bf16_f32 v113, v114, v115\n"
"v_cvt_pk_bf16_f32 v114, v116, v117\n"
"v_cvt_pk_bf16_f32 v115, v118, v119\n"
"buffer_store_dwordx4 v[112:115], v246, s[52:55], s91 offen offset:3072 sc1\n"
"v_cmp_gt_u32 vcc, 4, v252\n"
"s_cbranch_vccnz .Lslow_c13\n"
".Lret_c13:\n"
"ds_read_b32 v250, v248\n"
"s_waitcnt vmcnt(18)\n"
"ds_read_b64_tr_b16 v[128:129], v244 offset:32768\n"
"ds_read_b64_tr_b16 v[130:131], v244 offset:33792\n"
"ds_read_b64_tr_b16 v[132:133], v245 offset:32768\n"
"ds_read_b64_tr_b16 v[134:135], v245 offset:33792\n"
"s_waitcnt vmcnt(16)\n"
"ds_read_b64_tr_b16 v[136:137], v244 offset:34816\n"
"ds_read_b64_tr_b16 v[138:139], v244 offset:35840\n"
"ds_read_b64_tr_b16 v[140:141], v245 offset:34816\n"
"ds_read_b64_tr_b16 v[142:143], v245 offset:35840\n"
"s_mul_i32 s75, s64, 0x38100\n"
"s_mul_i32 s77, s97, 0x7000\n"
"s_add_u32 s75, s75, s77\n"
"s_add_u32 s75, s75, 0x400000\n"
"v_lshl_add_u32 v246, v232, 4, s75\n"
"v_add_u32 v247, 0x1c080, v246\n"
"s_lshl_b32 s75, s64, 12\n"
"s_mul_i32 s77, s97, 0x380\n"
"s_add_u32 s75, s75, s77\n"
"s_add_u32 s75, s75, 0xb23000\n"
"v_min_u32 v251, 6, v232\n"
"v_lshl_add_u32 v236, v251, 7, s75\n"
"s_waitcnt lgkmcnt(0)\n"
"v_cmp_gt_u32 vcc, 52, v250\n"
"s_cbranch_vccnz .Lslow_c14\n"
".Lret_c14:\n"
"v_add_u32 v248, 0x10000, v244\n"
"v_add_u32 v249, 0x10000, v245\n"
"v_readlane_b32 s86, v241, 6\n"
"s_add_u32 m0, s80, 0\n"
"s_nop 2\n"
"s_nop 0\n"
"buffer_load_dwordx4 v242, s[48:51], s86 offen lds\n"
"buffer_load_dwordx4 v243, s[48:51], s86 offen offset:1024 lds\n"
"s_waitcnt vmcnt(16)\n"
"ds_read_b64_tr_b16 v[144:145], v244 offset:49152\n"
"ds_read_b64_tr_b16 v[146:147], v244 offset:50176\n"
"ds_read_b64_tr_b16 v[148:149], v245 offset:49152\n"
"ds_read_b64_tr_b16 v[150:151], v245 offset:50176\n"
"v_readlane_b32 s87, v241, 7\n"
"s_add_u32 m0, s80, 0x800\n"
"s_nop 2\n"
"s_nop 0\n"
"buffer_load_dwordx4 v242, s[48:51], s87 offen lds\n"
"buffer_load_dwordx4 v243, s[48:51], s87 offen offset:1024 lds\n"
"s_waitcnt vmcnt(16)\n"
"ds_read_b64_tr_b16 v[152:153], v244 offset:51200\n"
"ds_read_b64_tr_b16 v[154:155], v244 offset:52224\n"
"ds_read_b64_tr_b16 v[156:157], v245 offset:51200\n"
"ds_read_b64_tr_b16 v[158:159], v245 offset:52224\n"
"v_readlane_b32 s86, v241, 8\n"
"s_add_u32 m0, s80, 0x4000\n"
"s_nop 2\n"
"s_nop 0\n"
"buffer_load_dwordx4 v242, s[48:51], s86 offen lds\n"
"buffer_load_dwordx4 v243, s[48:51], s86 offen offset:1024 lds\n"
"s_waitcnt vmcnt(16)\n"
"ds_read_b64_tr_b16 v[160:161], v248 offset:16384\n"
"ds_read_b64_tr_b16 v[162:163], v248 offset:17408\n"
"ds_read_b64_tr_b16 v[164:165], v249 offset:16384\n"
"ds_read_b64_tr_b16 v[166:167], v249 offset:17408\n"
"v_readlane_b32 s87, v241, 9\n"
"s_add_u32 m0, s80, 0x4800\n"
"s_nop 2\n"
"s_nop 0\n"
"buffer_load_dwordx4 v242, s[48:51], s87 offen lds\n"
"buffer_load_dwordx4 v243, s[48:51], s87 offen offset:1024 lds\n"
"s_waitcnt vmcnt(8)\n"
"s_mov_b64 exec, 1\n"
"buffer_store_dwordx2 v[238:239], v233, s[52:55], s93 offen sc1\n"
"s_mov_b64 exec, -1\n"
"ds_read_b64_tr_b16 v[168:169], v248 offset:18432\n"
"ds_read_b64_tr_b16 v[170:171], v248 offset:19456\n"
"ds_read_b64_tr_b16 v[172:173], v249 offset:18432\n"
"ds_read_b64_tr_b16 v[174:175], v249 offset:19456\n"
"v_readlane_b32 s86, v241, 10\n"
"s_add_u32 m0, s80, 0x10000\n"
"s_nop 2\n"
"s_nop 0\n"
"buffer_load_dwordx4 v242, s[48:51], s86 offen lds\n"
"buffer_load_dwordx4 v243, s[48:51], s86 offen offset:1024 lds\n"
"v_readlane_b32 s87, v241, 11\n"
"s_add_u32 m0, s80, 0x10800\n"
"s_nop 2\n"
"s_nop 0\n"
"buffer_load_dwordx4 v242, s[48:51], s87 offen lds\n"
"buffer_load_dwordx4 v243, s[48:51], s87 offen offset:1024 lds\n"
"v_readlane_b32 s86, v241, 12\n"
"s_add_u32 m0, s80, 0x8000\n"
"s_nop 2\n"
"s_nop 0\n"
"buffer_load_dwordx4 v242, s[48:51], s86 offen lds\n"
"buffer_load_dwordx4 v243, s[48:51], s86 offen offset:1024 lds\n"
"s_waitcnt vmcnt(13)\n"
"ds_read_b64_tr_b16 v[176:177], v244\n"
"ds_read_b64_tr_b16 v[178:179], v244 offset:1024\n"
"ds_read_b64_tr_b16 v[180:181], v245\n"
"ds_read_b64_tr_b16 v[182:183], v245 offset:1024\n"
"v_readlane_b32 s87, v241, 13\n"
"s_add_u32 m0, s80, 0x8800\n"
"s_nop 2\n"
"s_nop 0\n"
"buffer_load_dwordx4 v242, s[48:51], s87 offen lds\n"
"buffer_load_dwordx4 v243, s[48:51], s87 offen offset:1024 lds\n"
"s_waitcnt vmcnt(13)\n"
"ds_read_b64_tr_b16 v[184:185], v244 offset:2048\n"
"ds_read_b64_tr_b16 v[186:187], v244 offset:3072\n"
"ds_read_b64_tr_b16 v[188:189], v245 offset:2048\n"
"ds_read_b64_tr_b16 v[190:191], v245 offset:3072\n"
"s_waitcnt lgkmcnt(4)\n"
"v_readlane_b32 s86, v241, 14\n"
"s_add_u32 m0, s80, 0xc000\n"
"s_nop 2\n"
"s_nop 0\n"
"buffer_load_dwordx4 v242, s[48:51], s86 offen lds\n"
"buffer_load_dwordx4 v243, s[48:51], s86 offen offset:1024 lds\n"
"s_waitcnt vmcnt(13)\n"
"ds_read_b64_tr_b16 v[192:193], v244 offset:16384\n"
"ds_read_b64_tr_b16 v[194:195], v244 offset:17408\n"
"ds_read_b64_tr_b16 v[196:197], v245 offset:16384\n"
"ds_read_b64_tr_b16 v[198:199], v245 offset:17408\n"
"v_readlane_b32 s87, v241, 15\n"
"s_add_u32 m0, s80, 0xc800\n"
"s_nop 2\n"
"s_nop 0\n"
"buffer_load_dwordx4 v242, s[48:51], s87 offen lds\n"
"buffer_load_dwordx4 v243, s[48:51], s87 offen offset:1024 lds\n"
"s_waitcnt vmcnt(13)\n"
"ds_read_b64_tr_b16 v[200:201], v244 offset:18432\n"
"ds_read_b64_tr_b16 v[202:203], v244 offset:19456\n"
"ds_read_b64_tr_b16 v[204:205], v245 offset:18432\n"
"ds_read_b64_tr_b16 v[206:207], v245 offset:19456\n"
"v_readlane_b32 s86, v241, 16\n"
"s_add_u32 m0, s80, 0x14000\n"
"s_nop 2\n"
"s_nop 0\n"
"buffer_load_dwordx4 v242, s[48:51], s86 offen lds\n"
"buffer_load_dwordx4 v243, s[48:51], s86 offen offset:1024 lds\n"
"s_waitcnt vmcnt(12)\n"
"ds_read_b64_tr_b16 v[208:209], v248\n"
"ds_read_b64_tr_b16 v[210:211], v248 offset:1024\n"
"ds_read_b64_tr_b16 v[212:213], v249\n"
"ds_read_b64_tr_b16 v[214:215], v249 offset:1024\n"
"v_readlane_b32 s87, v241, 17\n"
"s_add_u32 m0, s80, 0x14800\n"
"s_nop 2\n"
"s_nop 0\n"
"buffer_load_dwordx4 v242, s[48:51], s87 offen lds\n"
"buffer_load_dwordx4 v243, s[48:51], s87 offen offset:1024 lds\n"
"s_waitcnt vmcnt(12)\n"
"ds_read_b64_tr_b16 v[216:217], v248 offset:2048\n"
"ds_read_b64_tr_b16 v[218:219], v248 offset:3072\n"
"ds_read_b64_tr_b16 v[220:221], v249 offset:2048\n"
"ds_read_b64_tr_b16 v[222:223], v249 offset:3072\n"
"v_readlane_b32 s86, v241, 18\n"
"s_add_u32 m0, s80, 0\n"
"s_nop 2\n"
"s_nop 0\n"
"buffer_load_dwordx4 v242, s[48:51], s86 offen lds\n"
"buffer_load_dwordx4 v243, s[48:51], s86 offen offset:1024 lds\n"
"buffer_load_dwordx2 v[234:235], v236, s[52:55], 0 offen sc1\n"
"s_waitcnt lgkmcnt(4)\n"
"v_readlane_b32 s87, v241, 19\n"
"s_add_u32 m0, s80, 0x800\n"
"s_nop 2\n"
"s_nop 0\n"
"buffer_load_dwordx4 v242, s[48:51], s87 offen lds\n"
"buffer_load_dwordx4 v243, s[48:51], s87 offen offset:1024 lds\n"
"v_readlane_b32 s86, v241, 20\n"
"s_add_u32 m0, s80, 0x4000\n"
"s_nop 2\n"
"s_nop 0\n"
"buffer_load_dwordx4 v242, s[48:51], s86 offen lds\n"
"buffer_load_dwordx4 v243, s[48:51], s86 offen offset:1024 lds\n"
"v_readlane_b32 s87, v241, 21\n"
"s_add_u32 m0, s80, 0x4800\n"
"s_nop 2\n"
"s_nop 0\n"
"buffer_load_dwordx4 v242, s[48:51], s87 offen lds\n"
"buffer_load_dwordx4 v243, s[48:51], s87 offen offset:1024 lds\n"
"v_readlane_b32 s86, v241, 22\n"
"s_add_u32 m0, s80, 0x10000\n"
"s_nop 2\n"
"s_nop 0\n"
"buffer_load_dwordx4 v242, s[48:51], s86 offen lds\n"
"buffer_load_dwordx4 v243, s[48:51], s86 offen offset:1024 lds\n"
"s_waitcnt lgkmcnt(0)\n"
"v_readlane_b32 s87, v241, 23\n"
"s_add_u32 m0, s80, 0x10800\n"
"s_nop 2\n"
"s_nop 0\n"
"buffer_load_dwordx4 v242, s[48:51], s87 offen lds\n"
"buffer_load_dwordx4 v243, s[48:51], s87 offen offset:1024 lds\n"
"s_waitcnt vmcnt(10)\n"
"v_cmp_ne_u64 vcc, s[60:61], v[234:235]\n"
"s_cbranch_vccnz .Lslow_g15\n"
".Lret_g15:\n"
"v_readlane_b32 s84, v240, 0\n"
"s_nop 4\n"
"buffer_load_dwordx4 v[224:227], v246, s[52:55], s84 offen sc1\n"
"buffer_load_dwordx4 v[228:231], v247, s[52:55], s84 offen sc1\n"
"buffer_load_dwordx4 v[64:67], v246, s[52:55], s84 offen offset:1024 sc1\n"
"buffer_load_dwordx4 v[68:71], v247, s[52:55], s84 offen offset:1024 sc1\n"
"buffer_load_dwordx4 v[72:75], v246, s[52:55], s84 offen offset:2048 sc1\n"
"buffer_load_dwordx4 v[76:79], v247, s[52:55], s84 offen offset:2048 sc1\n"
"buffer_load_dwordx4 v[80:83], v246, s[52:55], s84 offen offset:3072 sc1\n"
"buffer_load_dwordx4 v[84:87], v247, s[52:55], s84 offen offset:3072 sc1\n"
"v_readlane_b32 s85, v240, 1\n"
"s_nop 4\n"
"buffer_load_dwordx4 v[88:91], v246, s[52:55], s85 offen sc1\n"
"buffer_load_dwordx4 v[92:95], v247, s[52:55], s85 offen sc1\n"
"buffer_load_dwordx4 v[96:99], v246, s[52:55], s85 offen offset:1024 sc1\n"
"buffer_load_dwordx4 v[100:103], v247, s[52:55], s85 offen offset:1024 sc1\n"
"buffer_load_dwordx4 v[104:107], v246, s[52:55], s85 offen offset:2048 sc1\n"
"buffer_load_dwordx4 v[108:111], v247, s[52:55], s85 offen offset:2048 sc1\n"
"buffer_load_dwordx4 v[112:115], v246, s[52:55], s85 offen offset:3072 sc1\n"
"buffer_load_dwordx4 v[116:119], v247, s[52:55], s85 offen offset:3072 sc1\n"
"v_readlane_b32 s84, v240, 2\n"
"s_nop 4\n"
"buffer_load_dwordx4 v[120:123], v246, s[52:55], s84 offen sc1\n"
"buffer_load_dwordx4 v[124:127], v247, s[52:55], s84 offen sc1\n"
"s_waitcnt vmcnt(17)\n"
"v_mfma_f32_32x32x16_bf16 v[0:15], v[128:131], v[224:227], 0\n"
"s_waitcnt vmcnt(16)\n"
"v_mfma_f32_32x32x16_bf16 v[32:47], v[128:131], v[228:231], 0\n"
"v_mfma_f32_32x32x16_bf16 v[16:31], v[132:135], v[224:227], 0\n"
"v_mfma_f32_32x32x16_bf16 v[48:63], v[132:135], v[228:231], 0\n"
"buffer_load_dwordx4 v[224:227], v246, s[52:55], s84 offen offset:1024 sc1\n"
"s_waitcnt vmcnt(16)\n"
"v_mfma_f32_32x32x16_bf16 v[0:15], v[136:139], v[64:67], v[0:15]\n"
"buffer_load_dwordx4 v[228:231], v247, s[52:55], s84 offen offset:1024 sc1\n"
"s_waitcnt vmcnt(16)\n"
"v_mfma_f32_32x32x16_bf16 v[32:47], v[136:139], v[68:71], v[32:47]\n"
"buffer_load_dwordx4 v[128:131], v246, s[52:55], s84 offen offset:2048 sc1\n"
"v_mfma_f32_32x32x16_bf16 v[16:31], v[140:143], v[64:67], v[16:31]\n"
"buffer_load_dwordx4 v[132:135], v247, s[52:55], s84 offen offset:2048 sc1\n"
"v_mfma_f32_32x32x16_bf16 v[48:63], v[140:143], v[68:71], v[48:63]\n"
"buffer_load_dwordx4 v[64:67], v246, s[52:55], s84 offen offset:3072 sc1\n"
"s_waitcnt vmcnt(18)\n"
"v_mfma_f32_32x32x16_bf16 v[0:15], v[144:147], v[72:75], v[0:15]\n"
"buffer_load_dwordx4 v[68:71], v247, s[52:55], s84 offen offset:3072 sc1\n"
"s_waitcnt vmcnt(18)\n"
"v_mfma_f32_32x32x16_bf16 v[32:47], v[144:147], v[76:79], v[32:47]\n"
"v_readlane_b32 s85, v240, 3\n"
"s_nop 4\n"
"buffer_load_dwordx4 v[136:139], v246, s[52:55], s85 offen sc1\n"
"v_mfma_f32_32x32x16_bf16 v[16:31], v[148:151], v[72:75], v[16:31]\n"
"buffer_load_dwordx4 v[140:143], v247, s[52:55], s85 offen sc1\n"
"v_mfma_f32_32x32x16_bf16 v[48:63], v[148:151], v[76:79], v[48:63]\n"
"buffer_load_dwordx4 v[72:75], v246, s[52:55], s85 offen offset:1024 sc1\n"
"s_waitcnt vmcnt(20)\n"
"v_mfma_f32_32x32x16_bf16 v[0:15], v[152:155], v[80:83], v[0:15]\n"
"buffer_load_dwordx4 v[76:79], v247, s[52:55], s85 offen offset:1024 sc1\n"
"s_waitcnt vmcnt(20)\n"
"v_mfma_f32_32x32x16_bf16 v[32:47], v[152:155], v[84:87], v[32:47]\n"
"buffer_load_dwordx4 v[144:147], v246, s[52:55], s85 offen offset:2048 sc1\n"
"v_mfma_f32_32x32x16_bf16 v[16:31], v[156:159], v[80:83], v[16:31]\n"
"buffer_load_dwordx4 v[148:151], v247, s[52:55], s85 offen offset:2048 sc1\n"
"v_mfma_f32_32x32x16_bf16 v[48:63], v[156:159], v[84:87], v[48:63]\n"
"buffer_load_dwordx4 v[80:83], v246, s[52:55], s85 offen offset:3072 sc1\n"
"s_waitcnt vmcnt(22)\n"
"v_mfma_f32_32x32x16_bf16 v[0:15], v[160:163], v[88:91], v[0:15]\n"
"buffer_load_dwordx4 v[84:87], v247, s[52:55], s85 offen offset:3072 sc1\n"
"s_waitcnt vmcnt(22)\n"
"v_mfma_f32_32x32x16_bf16 v[32:47], v[160:163], v[92:95], v[32:47]\n"
"v_readlane_b32 s84, v240, 4\n"
"s_nop 4\n"
"buffer_load_dwordx4 v[152:155], v246, s[52:55], s84 offen sc1\n"
"v_mfma_f32_32x32x16_bf16 v[16:31], v[164:167], v[88:91], v[16:31]\n"
"buffer_load_dwordx4 v[156:159], v247, s[52:55], s84 offen sc1\n"
"v_mfma_f32_32x32x16_bf16 v[48:63], v[164:167], v[92:95], v[48:63]\n"
"buffer_load_dwordx4 v[88:91], v246, s[52:55], s84 offen offset:1024 sc1\n"
"s_waitcnt vmcnt(24)\n"
"v_mfma_f32_32x32x16_bf16 v[0:15], v[168:171], v[96:99], v[0:15]\n"
"buffer_load_dwordx4 v[92:95], v247, s[52:55], s84 offen offset:1024 sc1\n"
"s_waitcnt vmcnt(24)\n"
"v_mfma_f32_32x32x16_bf16 v[32:47], v[168:171], v[100:103], v[32:47]\n"
"buffer_load_dwordx4 v[160:163], v246, s[52:55], s84 offen offset:2048 sc1\n"
"v_mfma_f32_32x32x16_bf16 v[16:31], v[172:175], v[96:99], v[16:31]\n"
"buffer_load_dwordx4 v[164:167], v247, s[52:55], s84 offen offset:2048 sc1\n"
"v_mfma_f32_32x32x16_bf16 v[48:63], v[172:175], v[100:103], v[48:63]\n"
"buffer_load_dwordx4 v[96:99], v246, s[52:55], s84 offen offset:3072 sc1\n"
"s_waitcnt vmcnt(26)\n"
"v_mfma_f32_32x32x16_bf16 v[0:15], v[176:179], v[104:107], v[0:15]\n"
"buffer_load_dwordx4 v[100:103], v247, s[52:55], s84 offen offset:3072 sc1\n"
"s_waitcnt vmcnt(26)\n"
"v_mfma_f32_32x32x16_bf16 v[32:47], v[176:179], v[108:111], v[32:47]\n"
"v_readlane_b32 s85, v240, 5\n"
"s_nop 4\n"
"buffer_load_dwordx4 v[168:171], v246, s[52:55], s85 offen sc1\n"
"v_mfma_f32_32x32x16_bf16 v[16:31], v[180:183], v[104:107], v[16:31]\n"
"buffer_load_dwordx4 v[172:175], v247, s[52:55], s85 offen sc1\n"
"v_mfma_f32_32x32x16_bf16 v[48:63], v[180:183], v[108:111], v[48:63]\n"
"buffer_load_dwordx4 v[104:107], v246, s[52:55], s85 offen offset:1024 sc1\n"
"s_waitcnt vmcnt(28)\n"
"v_mfma_f32_32x32x16_bf16 v[0:15], v[184:187], v[112:115], v[0:15]\n"
"buffer_load_dwordx4 v[108:111], v247, s[52:55], s85 offen offset:1024 sc1\n"
"s_waitcnt vmcnt(28)\n"
"v_mfma_f32_32x32x16_bf16 v[32:47], v[184:187], v[116:119], v[32:47]\n"
"buffer_load_dwordx4 v[176:179], v246, s[52:55], s85 offen offset:2048 sc1\n"
"v_mfma_f32_32x32x16_bf16 v[16:31], v[188:191], v[112:115], v[16:31]\n"
"buffer_load_dwordx4 v[180:183], v247, s[52:55], s85 offen offset:2048 sc1\n"
"v_mfma_f32_32x32x16_bf16 v[48:63], v[188:191], v[116:119], v[48:63]\n"
"buffer_load_dwordx4 v[112:115], v246, s[52:55], s85 offen offset:3072 sc1\n"
"s_waitcnt vmcnt(30)\n"
"v_mfma_f32_32x32x16_bf16 v[0:15], v[192:195], v[120:123], v[0:15]\n"
"buffer_load_dwordx4 v[116:119], v247, s[52:55], s85 offen offset:3072 sc1\n"
"s_waitcnt vmcnt(30)\n"
"v_mfma_f32_32x32x16_bf16 v[32:47], v[192:195], v[124:127], v[32:47]\n"
"v_readlane_b32 s84, v240, 6\n"
"s_nop 4\n"
"buffer_load_dwordx4 v[184:187], v246, s[52:55], s84 offen sc1\n"
"v_mfma_f32_32x32x16_bf16 v[16:31], v[196:199], v[120:123], v[16:31]\n"
"buffer_load_dwordx4 v[188:191], v247, s[52:55], s84 offen sc1\n"
"v_mfma_f32_32x32x16_bf16 v[48:63], v[196:199], v[124:127], v[48:63]\n"
"buffer_load_dwordx4 v[120:123], v246, s[52:55], s84 offen offset:1024 sc1\n"
"s_waitcnt vmcnt(32)\n"
"v_mfma_f32_32x32x16_bf16 v[0:15], v[200:203], v[224:227], v[0:15]\n"
"buffer_load_dwordx4 v[124:127], v247, s[52:55], s84 offen offset:1024 sc1\n"
"ds_read_b64_tr_b16 v[192:193], v244 offset:32768\n"
"ds_read_b64_tr_b16 v[194:195], v244 offset:33792\n"
"ds_read_b64_tr_b16 v[196:197], v245 offset:32768\n"
"s_waitcnt vmcnt(32)\n"
"v_mfma_f32_32x32x16_bf16 v[32:47], v[200:203], v[228:231], v[32:47]\n"
"ds_read_b64_tr_b16 v[198:199], v245 offset:33792\n"
"v_mfma_f32_32x32x16_bf16 v[16:31], v[204:207], v[224:227], v[16:31]\n"
"v_mfma_f32_32x32x16_bf16 v[48:63], v[204:207], v[228:231], v[48:63]\n"
"buffer_load_dwordx4 v[224:227], v246, s[52:55], s84 offen offset:2048 sc1\n"
"s_waitcnt vmcnt(32)\n"
"v_mfma_f32_32x32x16_bf16 v[0:15], v[208:211], v[128:131], v[0:15]\n"
"buffer_load_dwordx4 v[228:231], v247, s[52:55], s84 offen offset:2048 sc1\n"
"s_waitcnt lgkmcnt(0)\n"
"v_readlane_b32 s86, v241, 24\n"
"s_add_u32 m0, s80, 0x8000\n"
"s_nop 2\n"
"s_waitcnt vmcnt(32)\n"
"v_mfma_f32_32x32x16_bf16 v[32:47], v[208:211], v[132:135], v[32:47]\n"
"buffer_load_dwordx4 v242, s[48:51], s86 offen lds\n"
"buffer_load_dwordx4 v243, s[48:51], s86 offen offset:1024 lds\n"
"v_mfma_f32_32x32x16_bf16 v[16:31], v[212:215], v[128:131], v[16:31]\n"
"ds_read_b64_tr_b16 v[200:201], v244 offset:34816\n"
"ds_read_b64_tr_b16 v[202:203], v244 offset:35840\n"
"ds_read_b64_tr_b16 v[204:205], v245 offset:34816\n"
"ds_read_b64_tr_b16 v[206:207], v245 offset:35840\n"
"v_mfma_f32_32x32x16_bf16 v[48:63], v[212:215], v[132:135], v[48:63]\n"
"buffer_load_dwordx4 v[128:131], v246, s[52:55], s84 offen offset:3072 sc1\n"
"s_waitcnt vmcnt(34)\n"
"v_mfma_f32_32x32x16_bf16 v[0:15], v[216:219], v[64:67], v[0:15]\n"
"buffer_load_dwordx4 v[132:135], v247, s[52:55], s84 offen offset:3072 sc1\n"
"s_waitcnt lgkmcnt(0)\n"
"v_readlane_b32 s87, v241, 25\n"
"s_add_u32 m0, s80, 0x8800\n"
"s_nop 2\n"
"s_waitcnt vmcnt(34)\n"
"v_mfma_f32_32x32x16_bf16 v[32:47], v[216:219], v[68:71], v[32:47]\n"
"buffer_load_dwordx4 v242, s[48:51], s87 offen lds\n"
"buffer_load_dwordx4 v243, s[48:51], s87 offen offset:1024 lds\n"
"v_mfma_f32_32x32x16_bf16 v[16:31], v[220:223], v[64:67], v[16:31]\n"
"ds_read_b64_tr_b16 v[208:209], v244 offset:49152\n"
"ds_read_b64_tr_b16 v[210:211], v244 offset:50176\n"
"ds_read_b64_tr_b16 v[212:213], v245 offset:49152\n"
"ds_read_b64_tr_b16 v[214:215], v245 offset:50176\n"
"v_mfma_f32_32x32x16_bf16 v[48:63], v[220:223], v[68:71], v[48:63]\n"
"s_waitcnt lgkmcnt(0)\n"
"v_readlane_b32 s86, v241, 26\n"
"s_add_u32 m0, s80, 0xc000\n"
"s_nop 2\n"
"s_nop 0\n"
"buffer_load_dwordx4 v242, s[48:51], s86 offen lds\n"
"s_waitcnt vmcnt(36)\n"
"v_mfma_f32_32x32x16_bf16 v[0:15], v[192:195], v[136:139], v[0:15]\n"
"buffer_load_dwordx4 v243, s[48:51], s86 offen offset:1024 lds\n"
"ds_read_b64_tr_b16 v[64:65], v244 offset:51200\n"
"ds_read_b64_tr_b16 v[66:67], v244 offset:52224\n"
"ds_read_b64_tr_b16 v[68:69], v245 offset:51200\n"
"s_waitcnt vmcnt(36)\n"
"v_mfma_f32_32x32x16_bf16 v[32:47], v[192:195], v[140:143], v[32:47]\n"
"ds_read_b64_tr_b16 v[70:71], v245 offset:52224\n"
"s_waitcnt lgkmcnt(0)\n"
"v_readlane_b32 s87, v241, 27\n"
"s_add_u32 m0, s80, 0xc800\n"
"s_nop 2\n"
"s_nop 0\n"
"buffer_load_dwordx4 v242, s[48:51], s87 offen lds\n"
"buffer_load_dwordx4 v243, s[48:51], s87 offen offset:1024 lds\n"
"v_mfma_f32_32x32x16_bf16 v[16:31], v[196:199], v[136:139], v[16:31]\n"
"ds_read_b64_tr_b16 v[216:217], v248 offset:16384\n"
"ds_read_b64_tr_b16 v[218:219], v248 offset:17408\n"
"ds_read_b64_tr_b16 v[220:221], v249 offset:16384\n"
"ds_read_b64_tr_b16 v[222:223], v249 offset:17408\n"
"v_mfma_f32_32x32x16_bf16 v[48:63], v[196:199], v[140:143], v[48:63]\n"
"ds_read_b64_tr_b16 v[136:137], v248 offset:18432\n"
"ds_read_b64_tr_b16 v[138:139], v248 offset:19456\n"
"ds_read_b64_tr_b16 v[140:141], v249 offset:18432\n"
"ds_read_b64_tr_b16 v[142:143], v249 offset:19456\n"
"ds_read_b64_tr_b16 v[192:193], v244\n"
"s_waitcnt vmcnt(37)\n"
"v_mfma_f32_32x32x16_bf16 v[0:15], v[200:203], v[72:75], v[0:15]\n"
"ds_read_b64_tr_b16 v[194:195], v244 offset:1024\n"
"ds_read_b64_tr_b16 v[196:197], v245\n"
"ds_read_b64_tr_b16 v[198:199], v245 offset:1024\n"
"s_waitcnt vmcnt(36)\n"
"v_mfma_f32_32x32x16_bf16 v[32:47], v[200:203], v[76:79], v[32:47]\n"
"v_mfma_f32_32x32x16_bf16 v[16:31], v[204:207], v[72:75], v[16:31]\n"
"v_mfma_f32_32x32x16_bf16 v[48:63], v[204:207], v[76:79], v[48:63]\n"
"ds_read_b64_tr_b16 v[72:73], v244 offset:2048\n"
"ds_read_b64_tr_b16 v[74:75], v244 offset:3072\n"
"ds_read_b64_tr_b16 v[76:77], v245 offset:2048\n"
"ds_read_b64_tr_b16 v[78:79], v245 offset:3072\n"
"ds_read_b64_tr_b16 v[200:201], v244 offset:16384\n"
"s_waitcnt vmcnt(35)\n"
"v_mfma_f32_32x32x16_bf16 v[0:15], v[208:211], v[144:147], v[0:15]\n"
"ds_read_b64_tr_b16 v[202:203], v244 offset:17408\n"
"ds_read_b64_tr_b16 v[204:205], v245 offset:16384\n"
"ds_read_b64_tr_b16 v[206:207], v245 offset:17408\n"
"s_waitcnt vmcnt(34)\n"
"v_mfma_f32_32x32x16_bf16 v[32:47], v[208:211], v[148:151], v[32:47]\n"
"v_mfma_f32_32x32x16_bf16 v[16:31], v[212:215], v[144:147], v[16:31]\n"
"v_mfma_f32_32x32x16_bf16 v[48:63], v[212:215], v[148:151], v[48:63]\n"
"ds_read_b64_tr_b16 v[144:145], v244 offset:18432\n"
"ds_read_b64_tr_b16 v[146:147], v244 offset:19456\n"
"ds_read_b64_tr_b16 v[148:149], v245 offset:18432\n"
"ds_read_b64_tr_b16 v[150:151], v245 offset:19456\n"
"ds_read_b64_tr_b16 v[208:209], v248\n"
"s_waitcnt vmcnt(33)\n"
"v_mfma_f32_32x32x16_bf16 v[0:15], v[64:67], v[80:83], v[0:15]\n"
"ds_read_b64_tr_b16 v[210:211], v248 offset:1024\n"
"ds_read_b64_tr_b16 v[212:213], v249\n"
"ds_read_b64_tr_b16 v[214:215], v249 offset:1024\n"
"v_mov_b32 v255, 0x27e78\n"
"s_waitcnt vmcnt(32)\n"
"v_mfma_f32_32x32x16_bf16 v[32:47], v[64:67], v[84:87], v[32:47]\n"
"s_mov_b64 exec, 1\n"
"ds_write_b32 v255, v255\n"
"s_mov_b64 exec, -1\n"
"v_mfma_f32_32x32x16_bf16 v[16:31], v[68:71], v[80:83], v[16:31]\n"
"v_mfma_f32_32x32x16_bf16 v[48:63], v[68:71], v[84:87], v[48:63]\n"
"ds_read_b64_tr_b16 v[80:81], v248 offset:2048\n"
"ds_read_b64_tr_b16 v[82:83], v248 offset:3072\n"
"ds_read_b64_tr_b16 v[84:85], v249 offset:2048\n"
"ds_read_b64_tr_b16 v[86:87], v249 offset:3072\n"
"s_waitcnt vmcnt(8)\n"
"ds_read_b64_tr_b16 v[64:65], v244 offset:32768\n"
"s_waitcnt lgkmcnt(6)\n"
"v_mfma_f32_32x32x16_bf16 v[0:15], v[216:219], v[152:155], v[0:15]\n"
"ds_read_b64_tr_b16 v[66:67], v244 offset:33792\n"
"ds_read_b64_tr_b16 v[68:69], v245 offset:32768\n"
"ds_read_b64_tr_b16 v[70:71], v245 offset:33792\n"
"v_mfma_f32_32x32x16_bf16 v[32:47], v[216:219], v[156:159], v[32:47]\n"
"v_mfma_f32_32x32x16_bf16 v[16:31], v[220:223], v[152:155], v[16:31]\n"
"v_mfma_f32_32x32x16_bf16 v[48:63], v[220:223], v[156:159], v[48:63]\n"
"s_waitcnt vmcnt(4)\n"
"ds_read_b64_tr_b16 v[152:153], v244 offset:34816\n"
"ds_read_b64_tr_b16 v[154:155], v244 offset:35840\n"
"ds_read_b64_tr_b16 v[156:157], v245 offset:34816\n"
"ds_read_b64_tr_b16 v[158:159], v245 offset:35840\n"
"s_waitcnt vmcnt(2)\n"
"ds_read_b64_tr_b16 v[216:217], v244 offset:49152\n"
"v_mfma_f32_32x32x16_bf16 v[0:15], v[136:139], v[88:91], v[0:15]\n"
"ds_read_b64_tr_b16 v[218:219], v244 offset:50176\n"
"ds_read_b64_tr_b16 v[220:221], v245 offset:49152\n"
"ds_read_b64_tr_b16 v[222:223], v245 offset:50176\n"
"v_mfma_f32_32x32x16_bf16 v[32:47], v[136:139], v[92:95], v[32:47]\n"
"v_mfma_f32_32x32x16_bf16 v[16:31], v[140:143], v[88:91], v[16:31]\n"
"v_mfma_f32_32x32x16_bf16 v[48:63], v[140:143], v[92:95], v[48:63]\n"
"s_waitcnt vmcnt(0)\n"
"ds_read_b64_tr_b16 v[88:89], v244 offset:51200\n"
"ds_read_b64_tr_b16 v[90:91], v244 offset:52224\n"
"ds_read_b64_tr_b16 v[92:93], v245 offset:51200\n"
"ds_read_b64_tr_b16 v[94:95], v245 offset:52224\n"
"v_mfma_f32_32x32x16_bf16 v[0:15], v[192:195], v[160:163], v[0:15]\n"
"v_mfma_f32_32x32x16_bf16 v[32:47], v[192:195], v[164:167], v[32:47]\n"
"v_mfma_f32_32x32x16_bf16 v[16:31], v[196:199], v[160:163], v[16:31]\n"
"v_mfma_f32_32x32x16_bf16 v[48:63], v[196:199], v[164:167], v[48:63]\n"
"v_mfma_f32_32x32x16_bf16 v[0:15], v[72:75], v[96:99], v[0:15]\n"
"v_mfma_f32_32x32x16_bf16 v[32:47], v[72:75], v[100:103], v[32:47]\n"
"v_mfma_f32_32x32x16_bf16 v[16:31], v[76:79], v[96:99], v[16:31]\n"
"v_mfma_f32_32x32x16_bf16 v[48:63], v[76:79], v[100:103], v[48:63]\n"
"s_lshl_b32 s76, s97, 12\n"
"s_add_u32 s76, s76, 0x8000\n"
"v_and_b32 v251, 31, v232\n"
"v_lshrrev_b32 v252, 5, v232\n"
"v_lshlrev_b32 v252, 1, v252\n"
"v_and_b32 v253, 7, v232\n"
"v_xor_b32 v252, v252, v253\n"
"v_mfma_f32_32x32x16_bf16 v[0:15], v[200:203], v[168:171], v[0:15]\n"
"v_lshlrev_b32 v252, 4, v252\n"
"v_lshl_add_u32 v251, v251, 7, v252\n"
"v_add_u32 v72, s76, v251\n"
"v_xor_b32 v73, 16, v72\n"
"v_xor_b32 v74, 64, v72\n"
"v_mfma_f32_32x32x16_bf16 v[32:47], v[200:203], v[172:175], v[32:47]\n"
"v_xor_b32 v75, 0x50, v72\n"
"s_lshl_b32 s76, s97, 2\n"
"v_lshrrev_b32 v251, 4, v232\n"
"v_add_u32 v251, s76, v251\n"
"v_and_b32 v252, 15, v232\n"
"s_mul_i32 s75, s64, 0xe000\n"
"s_lshl_b32 s77, s65, 7\n"
"v_mfma_f32_32x32x16_bf16 v[16:31], v[204:207], v[168:171], v[16:31]\n"
"s_add_u32 s75, s75, s77\n"
"v_mul_u32_u24 v253, 0x380, v251\n"
"v_lshl_add_u32 v253, v252, 3, v253\n"
"v_add_u32 v136, s75, v253\n"
"buffer_load_dwordx2 v[140:141], v136, s[40:43], 0 offen\n"
"v_mfma_f32_32x32x16_bf16 v[48:63], v[204:207], v[172:175], v[48:63]\n"
"v_lshlrev_b32 v253, 2, v251\n"
"v_add_u32 v138, 0x24800, v253\n"
"v_and_b32 v253, 15, v251\n"
"v_xor_b32 v253, v253, v252\n"
"v_lshlrev_b32 v253, 3, v253\n"
"v_mfma_f32_32x32x16_bf16 v[0:15], v[144:147], v[104:107], v[0:15]\n"
"v_lshl_add_u32 v253, v251, 7, v253\n"
"v_add_u32 v139, 0x22800, v253\n"
"v_lshrrev_b32 v253, 3, v252\n"
"v_lshlrev_b32 v253, 2, v253\n"
"v_and_b32 v254, 1, v252\n"
"v_mfma_f32_32x32x16_bf16 v[32:47], v[144:147], v[108:111], v[32:47]\n"
"v_lshl_add_u32 v253, v254, 1, v253\n"
"v_bfe_u32 v254, v252, 2, 1\n"
"v_or_b32 v253, v253, v254\n"
"v_and_b32 v254, 7, v251\n"
"v_xor_b32 v253, v253, v254\n"
"v_mfma_f32_32x32x16_bf16 v[16:31], v[148:151], v[104:107], v[16:31]\n"
"v_lshlrev_b32 v253, 4, v253\n"
"v_bfe_u32 v254, v252, 1, 1\n"
"v_lshl_add_u32 v253, v254, 3, v253\n"
"v_lshl_add_u32 v253, v251, 7, v253\n"
"v_add_u32 v137, 0x8000, v253\n"
"s_lshl_b32 s76, s97, 2\n"
"v_mfma_f32_32x32x16_bf16 v[48:63], v[148:151], v[108:111], v[48:63]\n"
"v_lshrrev_b32 v251, 4, v232\n"
"v_add_u32 v251, s76, v251\n"
"v_and_b32 v252, 15, v232\n"
"s_mul_i32 s75, s64, 0xe000\n"
"s_lshl_b32 s77, s65, 7\n"
"s_add_u32 s75, s75, s77\n"
"s_add_u32 s75, s75, 0x7000\n"
"v_mfma_f32_32x32x16_bf16 v[0:15], v[208:211], v[176:179], v[0:15]\n"
"v_mul_u32_u24 v253, 0x380, v251\n"
"v_lshl_add_u32 v253, v252, 3, v253\n"
"v_add_u32 v160, s75, v253\n"
"buffer_load_dwordx2 v[164:165], v160, s[40:43], 0 offen\n"
"v_lshlrev_b32 v253, 2, v251\n"
"v_mfma_f32_32x32x16_bf16 v[32:47], v[208:211], v[180:183], v[32:47]\n"
"v_add_u32 v162, 0x24880, v253\n"
"v_and_b32 v253, 15, v251\n"
"v_xor_b32 v253, v253, v252\n"
"v_lshlrev_b32 v253, 3, v253\n"
"v_lshl_add_u32 v253, v251, 7, v253\n"
"v_mfma_f32_32x32x16_bf16 v[16:31], v[212:215], v[176:179], v[16:31]\n"
"v_add_u32 v163, 0x23800, v253\n"
"v_lshrrev_b32 v253, 3, v252\n"
"v_lshlrev_b32 v253, 2, v253\n"
"v_and_b32 v254, 1, v252\n"
"v_lshl_add_u32 v253, v254, 1, v253\n"
"v_mfma_f32_32x32x16_bf16 v[48:63], v[212:215], v[180:183], v[48:63]\n"
"v_bfe_u32 v254, v252, 2, 1\n"
"v_or_b32 v253, v253, v254\n"
"v_and_b32 v254, 7, v251\n"
"v_xor_b32 v253, v253, v254\n"
"v_lshlrev_b32 v253, 4, v253\n"
"s_waitcnt lgkmcnt(0)\n"
"v_mfma_f32_32x32x16_bf16 v[0:15], v[80:83], v[112:115], v[0:15]\n"
"v_bfe_u32 v254, v252, 1, 1\n"
"v_lshl_add_u32 v253, v254, 3, v253\n"
"v_lshl_add_u32 v253, v251, 7, v253\n"
"v_add_u32 v161, 0xc000, v253\n"
"s_mov_b32 s82, 0x3f80\n"
"s_mov_b32 s83, 0x3f800000\n"
"v_mfma_f32_32x32x16_bf16 v[32:47], v[80:83], v[116:119], v[32:47]\n"
"v_mfma_f32_32x32x16_bf16 v[16:31], v[84:87], v[112:115], v[16:31]\n"
"v_mfma_f32_32x32x16_bf16 v[48:63], v[84:87], v[116:119], v[48:63]\n"
"v_mfma_f32_32x32x16_bf16 v[0:15], v[64:67], v[184:187], v[0:15]\n"
"v_mfma_f32_32x32x16_bf16 v[16:31], v[68:71], v[184:187], v[16:31]\n"
"v_mfma_f32_32x32x16_bf16 v[0:15], v[152:155], v[120:123], v[0:15]\n"
"v_mfma_f32_32x32x16_bf16 v[16:31], v[156:159], v[120:123], v[16:31]\n"
"v_mfma_f32_32x32x16_bf16 v[0:15], v[216:219], v[224:227], v[0:15]\n"
"v_mfma_f32_32x32x16_bf16 v[16:31], v[220:223], v[224:227], v[16:31]\n"
"v_mfma_f32_32x32x16_bf16 v[0:15], v[88:91], v[128:131], v[0:15]\n"
"v_mfma_f32_32x32x16_bf16 v[16:31], v[92:95], v[128:131], v[16:31]\n"
"v_mfma_f32_32x32x16_bf16 v[32:47], v[64:67], v[188:191], v[32:47]\n"
"v_mfma_f32_32x32x16_bf16 v[48:63], v[68:71], v[188:191], v[48:63]\n"
"s_nop 8\n"
"v_cvt_pk_bf16_f32 v0, v0, v1\n"
"v_cvt_pk_bf16_f32 v1, v2, v3\n"
"v_cvt_pk_bf16_f32 v2, v4, v5\n"
"v_cvt_pk_bf16_f32 v3, v6, v7\n"
"v_mfma_f32_32x32x16_bf16 v[32:47], v[152:155], v[124:127], v[32:47]\n"
"ds_write_b128 v72, v[0:3]\n"
"v_cvt_pk_bf16_f32 v8, v8, v9\n"
"v_mfma_f32_32x32x16_bf16 v[48:63], v[156:159], v[124:127], v[48:63]\n"
"v_cvt_pk_bf16_f32 v9, v10, v11\n"
"v_cvt_pk_bf16_f32 v10, v12, v13\n"
"v_cvt_pk_bf16_f32 v11, v14, v15\n"
"v_mfma_f32_32x32x16_bf16 v[32:47], v[216:219], v[228:231], v[32:47]\n"
"ds_write_b128 v73, v[8:11]\n"
"v_cvt_pk_bf16_f32 v16, v16, v17\n"
"v_mfma_f32_32x32x16_bf16 v[48:63], v[220:223], v[228:231], v[48:63]\n"
"v_cvt_pk_bf16_f32 v17, v18, v19\n"
"v_cvt_pk_bf16_f32 v18, v20, v21\n"
"v_cvt_pk_bf16_f32 v19, v22, v23\n"
"v_mfma_f32_32x32x16_bf16 v[32:47], v[88:91], v[132:135], v[32:47]\n"
"ds_write_b128 v74, v[16:19]\n"
"v_cvt_pk_bf16_f32 v24, v24, v25\n"
"v_mfma_f32_32x32x16_bf16 v[48:63], v[92:95], v[132:135], v[48:63]\n"
"v_cvt_pk_bf16_f32 v25, v26, v27\n"
"v_cvt_pk_bf16_f32 v26, v28, v29\n"
"v_cvt_pk_bf16_f32 v27, v30, v31\n"
"ds_write_b128 v75, v[24:27]\n"
"s_nop 4\n"
"v_cvt_pk_bf16_f32 v32, v32, v33\n"
"v_cvt_pk_bf16_f32 v33, v34, v35\n"
"v_cvt_pk_bf16_f32 v34, v36, v37\n"
"v_cvt_pk_bf16_f32 v35, v38, v39\n"
"ds_write_b128 v72, v[32:35] offset:16384\n"
"v_cvt_pk_bf16_f32 v40, v40, v41\n"
"v_cvt_pk_bf16_f32 v41, v42, v43\n"
"v_cvt_pk_bf16_f32 v42, v44, v45\n"
"v_cvt_pk_bf16_f32 v43, v46, v47\n"
"ds_write_b128 v73, v[40:43] offset:16384\n"
"v_cvt_pk_bf16_f32 v48, v48, v49\n"
"v_cvt_pk_bf16_f32 v49, v50, v51\n"
"v_cvt_pk_bf16_f32 v50, v52, v53\n"
"v_cvt_pk_bf16_f32 v51, v54, v55\n"
"ds_write_b128 v74, v[48:51] offset:16384\n"
"v_cvt_pk_bf16_f32 v56, v56, v57\n"
"v_cvt_pk_bf16_f32 v57, v58, v59\n"
"v_cvt_pk_bf16_f32 v58, v60, v61\n"
"v_cvt_pk_bf16_f32 v59, v62, v63\n"
"ds_write_b128 v75, v[56:59] offset:16384\n"
"s_waitcnt vmcnt(1)\n"
"v_lshlrev_b32 v192, 16, v140\n"
"v_and_b32 v193, s39, v140\n"
"v_lshlrev_b32 v194, 16, v141\n"
"v_and_b32 v195, s39, v141\n"
"s_waitcnt vmcnt(0)\n"
"v_lshlrev_b32 v96, 16, v164\n"
"v_and_b32 v97, s39, v164\n"
"v_lshlrev_b32 v98, 16, v165\n"
"v_and_b32 v99, s39, v165\n"
"s_waitcnt lgkmcnt(0)\n"
"s_barrier\n"
"ds_read_b64 v[0:1], v137\n"
"ds_read_b64 v[2:3], v137 offset:4096\n"
"ds_read_b64 v[4:5], v137 offset:8192\n"
"ds_read_b64 v[6:7], v137 offset:12288\n"
"ds_read_b64 v[8:9], v139\n"
"ds_read_b32 v142, v138\n"
"ds_read_b64 v[16:17], v161\n"
"ds_read_b64 v[18:19], v161 offset:4096\n"
"ds_read_b64 v[20:21], v161 offset:8192\n"
"ds_read_b64 v[22:23], v161 offset:12288\n"
"ds_read_b64 v[24:25], v163\n"
"ds_read_b32 v166, v162\n"
"s_waitcnt lgkmcnt(11)\n"
"v_lshlrev_b32 v12, 16, v0\n"
"v_and_b32 v13, s39, v0\n"
"v_lshlrev_b32 v14, 16, v1\n"
"v_and_b32 v15, s39, v1\n"
"s_waitcnt lgkmcnt(7)\n"
"v_dot2_f32_bf16 v12, v2, s82, v12\n"
"v_dot2_f32_bf16 v13, v2, s83, v13\n"
"v_dot2_f32_bf16 v14, v3, s82, v14\n"
"v_dot2_f32_bf16 v15, v3, s83, v15\n"
"v_dot2_f32_bf16 v12, v4, s82, v12\n"
"v_dot2_f32_bf16 v13, v4, s83, v13\n"
"v_dot2_f32_bf16 v14, v5, s82, v14\n"
"v_dot2_f32_bf16 v15, v5, s83, v15\n"
"v_dot2_f32_bf16 v12, v6, s82, v12\n"
"v_dot2_f32_bf16 v13, v6, s83, v13\n"
"v_dot2_f32_bf16 v14, v7, s82, v14\n"
"v_dot2_f32_bf16 v15, v7, s83, v15\n"
"v_dot2_f32_bf16 v192, v8, s82, v192\n"
"v_dot2_f32_bf16 v193, v8, s83, v193\n"
"v_dot2_f32_bf16 v194, v9, s82, v194\n"
"v_dot2_f32_bf16 v195, v9, s83, v195\n"
"s_waitcnt lgkmcnt(0)\n"
"v_fma_f32 v12, v12, v142, v192\n"
"v_fma_f32 v13, v13, v142, v193\n"
"v_fma_f32 v14, v14, v142, v194\n"
"v_fma_f32 v15, v15, v142, v195\n"
"v_cvt_pk_bf16_f32 v12, v12, v13\n"
"v_cvt_pk_bf16_f32 v13, v14, v15\n"
"buffer_store_dwordx2 v[12:13], v136, s[56:59], 0 offen sc1\n"
"v_lshlrev_b32 v28, 16, v16\n"
"v_and_b32 v29, s39, v16\n"
"v_lshlrev_b32 v30, 16, v17\n"
"v_and_b32 v31, s39, v17\n"
"v_dot2_f32_bf16 v28, v18, s82, v28\n"
"v_dot2_f32_bf16 v29, v18, s83, v29\n"
"v_dot2_f32_bf16 v30, v19, s82, v30\n"
"v_dot2_f32_bf16 v31, v19, s83, v31\n"
"v_dot2_f32_bf16 v28, v20, s82, v28\n"
"v_dot2_f32_bf16 v29, v20, s83, v29\n"
"v_dot2_f32_bf16 v30, v21, s82, v30\n"
"v_dot2_f32_bf16 v31, v21, s83, v31\n"
"v_dot2_f32_bf16 v28, v22, s82, v28\n"
"v_dot2_f32_bf16 v29, v22, s83, v29\n"
"v_dot2_f32_bf16 v30, v23, s82, v30\n"
"v_dot2_f32_bf16 v31, v23, s83, v31\n"
"v_dot2_f32_bf16 v96, v24, s82, v96\n"
"v_dot2_f32_bf16 v97, v24, s83, v97\n"
"v_dot2_f32_bf16 v98, v25, s82, v98\n"
"v_dot2_f32_bf16 v99, v25, s83, v99\n"
"v_fma_f32 v28, v28, v166, v96\n"
"v_fma_f32 v29, v29, v166, v97\n"
"v_fma_f32 v30, v30, v166, v98\n"
"v_fma_f32 v31, v31, v166, v99\n"
"v_cvt_pk_bf16_f32 v28, v28, v29\n"
"v_cvt_pk_bf16_f32 v29, v30, v31\n"
"buffer_store_dwordx2 v[28:29], v160, s[56:59], 0 offen sc1\n"
"s_endpgm\n"
".Lswave:\n"
"s_lshl_b32 s70, s96, 1\n"
"s_lshl_b32 s77, s67, 0\n"
"s_add_u32 s70, s70, s77\n"
"s_lshl_b32 s75, s70, 9\n"
"s_lshl_b32 s77, s66, 8\n"
"s_add_u32 s75, s75, s77\n"
"v_lshl_add_u32 v254, v232, 2, s75\n"
"s_lshl_b32 s76, s96, 2\n"
"s_waitcnt lgkmcnt(0)\n"
"s_load_dword s88, s[26:27], s76\n"
"s_load_dword s68, s[28:29], s76\n"
"global_load_dword v152, v254, s[18:19]\n"
"global_load_dword v153, v254, s[20:21]\n"
"global_load_dword v154, v254, s[22:23]\n"
"global_load_dword v155, v254, s[24:25]\n"
"s_and_b32 s53, s53, 0xffff\n"
"s_mov_b32 s54, 0xbe3000\n"
"s_mov_b32 s55, 0x20000\n"
"s_and_b32 s49, s49, 0xffff\n"
"s_mov_b32 s50, 0x188000\n"
"s_mov_b32 s51, 0x20000\n"
"s_mov_b32 s40, s16\n"
"s_and_b32 s41, s17, 0xffff\n"
"s_mov_b32 s42, 0x1c0000\n"
"s_mov_b32 s43, 0x20000\n"
"s_and_b32 s57, s57, 0xffff\n"
"s_mov_b32 s58, 0x1c0000\n"
"s_mov_b32 s59, 0x20000\n"
"s_mul_i32 s91, s97, 0x2200\n"
"s_add_u32 s91, s91, 0x18000\n"
"v_lshrrev_b32 v184, 5, v232\n"
"v_bfe_u32 v185, v232, 2, 2\n"
"v_and_b32 v186, 3, v232\n"
"v_bfe_u32 v187, v232, 4, 1\n"
"v_and_b32 v251, 7, v232\n"
"v_lshlrev_b32 v251, 4, v251\n"
"v_lshl_add_u32 v251, v232, 7, v251\n"
"v_add_u32 v189, s91, v251\n"
"v_lshrrev_b32 v251, 1, v185\n"
"v_lshl_add_u32 v251, v184, 2, v251\n"
"v_and_b32 v252, 1, v185\n"
"v_lshlrev_b32 v252, 2, v252\n"
"v_lshl_add_u32 v252, v187, 1, v252\n"
"v_lshrrev_b32 v253, 1, v186\n"
"v_or_b32 v252, v252, v253\n"
"v_xor_b32 v252, v252, v251\n"
"v_lshlrev_b32 v252, 4, v252\n"
"v_lshl_add_u32 v252, v251, 7, v252\n"
"v_and_b32 v253, 1, v186\n"
"v_lshl_add_u32 v252, v253, 3, v252\n"
"v_add_u32 v190, s91, v252\n"
"v_xor_b32 v191, 32, v190\n"
"v_add_u32 v191, 0x100, v191\n"
"s_lshl_b32 s75, s67, 4\n"
"s_add_u32 s75, s75, 0x27e50\n"
"v_mov_b32 v248, s75\n"
"s_mul_i32 s76, s66, 0xa00\n"
"s_add_u32 s98, s98, s76\n"
"s_addc_u32 s99, s99, 0\n"
"v_lshlrev_b32 v254, 4, v232\n"
"v_lshlrev_b32 v255, 3, v232\n"
"s_lshl_b32 s75, s67, 4\n"
"s_sub_u32 s75, 16, s75\n"
"s_waitcnt lgkmcnt(0)\n"
"v_mov_b32 v192, s88\n"
"v_mov_b32 v193, s68\n"
"v_lshlrev_b32 v192, s75, v192\n"
"v_lshlrev_b32 v253, s75, v193\n"
"v_and_b32 v192, 0xffff0000, v192\n"
"v_and_b32 v253, 0xffff0000, v253\n"
"v_mul_f32 v192, 0x3fb8aa3b, v192\n"
"v_readfirstlane_b32 s89, v253\n"
"v_exp_f32 v192, v192\n"
"s_cmp_eq_u32 s66, 0\n"
"s_cselect_b32 s89, s89, 0\n"
"v_cvt_pk_bf16_f32 v192, v192, v192\n"
"v_and_b32 v192, 0xffff0000, v192\n"
"s_barrier\n"
"s_waitcnt vmcnt(3)\n"
"v_lshlrev_b32 v156, 16, v152\n"
"s_waitcnt vmcnt(2)\n"
"v_lshlrev_b32 v157, 16, v153\n"
"v_mul_f32 v156, 0x3fb8aa3b, v156\n"
"v_mul_f32 v157, v192, v157\n"
"v_exp_f32 v156, v156\n"
"v_mul_f32 v157, 0x3e22f983, v157\n"
"s_waitcnt vmcnt(1)\n"
"v_lshlrev_b32 v164, 16, v154\n"
"v_cvt_pk_bf16_f32 v156, v156, v156\n"
"s_waitcnt vmcnt(0)\n"
"v_lshlrev_b32 v165, 16, v155\n"
"v_and_b32 v156, 0xffff0000, v156\n"
"v_mul_f32 v164, v192, v164\n"
"v_mul_f32 v156, v192, v156\n"
"v_mul_f32 v165, v192, v165\n"
"v_mul_f32 v156, 0xbfb8aa3b, v156\n"
"v_add_f32 v164, v164, v164\n"
"v_add_f32 v165, v165, v165\n"
"v_mul_f32 v172, 0x41000000, v156\n"
"v_mul_f32 v173, 0x41000000, v157\n"
"v_exp_f32 v172, v172\n"
"v_fract_f32 v173, v173\n"
"v_cos_f32 v160, v173\n"
"v_sin_f32 v161, v173\n"
"v_mul_f32 v160, v172, v160\n"
"v_mul_f32 v161, v172, v161\n"
"v_pk_mul_f32 v[172:173], v[164:165], v[160:161] op_sel:[1,1] op_sel_hi:[1,0]\n"
"v_pk_mul_f32 v[174:175], v[160:161], v[160:161] op_sel:[1,1] op_sel_hi:[1,0]\n"
"v_pk_fma_f32 v[166:167], v[164:165], v[160:161], v[172:173] op_sel_hi:[0,1,1] neg_lo:[0,0,1]\n"
"v_pk_fma_f32 v[158:159], v[160:161], v[160:161], v[174:175] op_sel_hi:[0,1,1] neg_lo:[0,0,1]\n"
"v_pk_mul_f32 v[172:173], v[164:165], v[158:159] op_sel:[1,1] op_sel_hi:[1,0]\n"
"v_pk_mul_f32 v[174:175], v[166:167], v[158:159] op_sel:[1,1] op_sel_hi:[1,0]\n"
"v_pk_fma_f32 v[168:169], v[164:165], v[158:159], v[172:173] op_sel_hi:[0,1,1] neg_lo:[0,0,1]\n"
"v_pk_fma_f32 v[170:171], v[166:167], v[158:159], v[174:175] op_sel_hi:[0,1,1] neg_lo:[0,0,1]\n"
"v_cvt_pk_bf16_f32 v176, v164, v166\n"
"v_cvt_pk_bf16_f32 v180, -v165, -v167\n"
"v_pk_mul_f32 v[172:173], v[168:169], v[158:159] op_sel:[1,1] op_sel_hi:[1,0]\n"
"v_pk_mul_f32 v[174:175], v[170:171], v[158:159] op_sel:[1,1] op_sel_hi:[1,0]\n"
"v_pk_fma_f32 v[164:165], v[168:169], v[158:159], v[172:173] op_sel_hi:[0,1,1] neg_lo:[0,0,1]\n"
"v_pk_fma_f32 v[166:167], v[170:171], v[158:159], v[174:175] op_sel_hi:[0,1,1] neg_lo:[0,0,1]\n"
"v_cvt_pk_bf16_f32 v177, v168, v170\n"
"v_cvt_pk_bf16_f32 v181, -v169, -v171\n"
"v_pk_mul_f32 v[172:173], v[164:165], v[158:159] op_sel:[1,1] op_sel_hi:[1,0]\n"
"v_pk_mul_f32 v[174:175], v[166:167], v[158:159] op_sel:[1,1] op_sel_hi:[1,0]\n"
"v_pk_fma_f32 v[168:169], v[164:165], v[158:159], v[172:173] op_sel_hi:[0,1,1] neg_lo:[0,0,1]\n"
"v_pk_fma_f32 v[170:171], v[166:167], v[158:159], v[174:175] op_sel_hi:[0,1,1] neg_lo:[0,0,1]\n"
"v_cvt_pk_bf16_f32 v178, v164, v166\n"
"v_cvt_pk_bf16_f32 v182, -v165, -v167\n"
"v_pk_mul_f32 v[172:173], v[168:169], v[158:159] op_sel:[1,1] op_sel_hi:[1,0]\n"
"v_pk_mul_f32 v[174:175], v[170:171], v[158:159] op_sel:[1,1] op_sel_hi:[1,0]\n"
"v_pk_fma_f32 v[164:165], v[168:169], v[158:159], v[172:173] op_sel_hi:[0,1,1] neg_lo:[0,0,1]\n"
"v_pk_fma_f32 v[166:167], v[170:171], v[158:159], v[174:175] op_sel_hi:[0,1,1] neg_lo:[0,0,1]\n"
"v_cvt_pk_bf16_f32 v179, v168, v170\n"
"v_cvt_pk_bf16_f32 v183, -v169, -v171\n"
"ds_write_b128 v189, v[176:179]\n"
"v_pk_mul_f32 v[172:173], v[164:165], v[158:159] op_sel:[1,1] op_sel_hi:[1,0]\n"
"v_pk_mul_f32 v[174:175], v[166:167], v[158:159] op_sel:[1,1] op_sel_hi:[1,0]\n"
"v_pk_fma_f32 v[168:169], v[164:165], v[158:159], v[172:173] op_sel_hi:[0,1,1] neg_lo:[0,0,1]\n"
"v_pk_fma_f32 v[170:171], v[166:167], v[158:159], v[174:175] op_sel_hi:[0,1,1] neg_lo:[0,0,1]\n"
"v_cvt_pk_bf16_f32 v176, v164, v166\n"
"v_cvt_pk_bf16_f32 v144, -v165, -v167\n"
"v_pk_mul_f32 v[172:173], v[168:169], v[158:159] op_sel:[1,1] op_sel_hi:[1,0]\n"
"v_pk_mul_f32 v[174:175], v[170:171], v[158:159] op_sel:[1,1] op_sel_hi:[1,0]\n"
"v_pk_fma_f32 v[164:165], v[168:169], v[158:159], v[172:173] op_sel_hi:[0,1,1] neg_lo:[0,0,1]\n"
"v_pk_fma_f32 v[166:167], v[170:171], v[158:159], v[174:175] op_sel_hi:[0,1,1] neg_lo:[0,0,1]\n"
"v_cvt_pk_bf16_f32 v177, v168, v170\n"
"v_cvt_pk_bf16_f32 v145, -v169, -v171\n"
"v_xor_b32 v188, 64, v189\n"
"ds_write_b128 v188, v[180:183]\n"
"v_pk_mul_f32 v[172:173], v[164:165], v[158:159] op_sel:[1,1] op_sel_hi:[1,0]\n"
"v_pk_mul_f32 v[174:175], v[166:167], v[158:159] op_sel:[1,1] op_sel_hi:[1,0]\n"
"v_pk_fma_f32 v[168:169], v[164:165], v[158:159], v[172:173] op_sel_hi:[0,1,1] neg_lo:[0,0,1]\n"
"v_pk_fma_f32 v[170:171], v[166:167], v[158:159], v[174:175] op_sel_hi:[0,1,1] neg_lo:[0,0,1]\n"
"v_cvt_pk_bf16_f32 v178, v164, v166\n"
"v_cvt_pk_bf16_f32 v146, -v165, -v167\n"
"v_pk_mul_f32 v[172:173], v[168:169], v[158:159] op_sel:[1,1] op_sel_hi:[1,0]\n"
"v_pk_mul_f32 v[174:175], v[170:171], v[158:159] op_sel:[1,1] op_sel_hi:[1,0]\n"
"v_pk_fma_f32 v[164:165], v[168:169], v[158:159], v[172:173] op_sel_hi:[0,1,1] neg_lo:[0,0,1]\n"
"v_pk_fma_f32 v[166:167], v[170:171], v[158:159], v[174:175] op_sel_hi:[0,1,1] neg_lo:[0,0,1]\n"
"v_cvt_pk_bf16_f32 v179, v168, v170\n"
"v_cvt_pk_bf16_f32 v147, -v169, -v171\n"
"v_xor_b32 v188, 16, v189\n"
"ds_write_b128 v188, v[176:179]\n"
"v_pk_mul_f32 v[172:173], v[164:165], v[158:159] op_sel:[1,1] op_sel_hi:[1,0]\n"
"v_pk_mul_f32 v[174:175], v[166:167], v[158:159] op_sel:[1,1] op_sel_hi:[1,0]\n"
"v_pk_fma_f32 v[168:169], v[164:165], v[158:159], v[172:173] op_sel_hi:[0,1,1] neg_lo:[0,0,1]\n"
"v_pk_fma_f32 v[170:171], v[166:167], v[158:159], v[174:175] op_sel_hi:[0,1,1] neg_lo:[0,0,1]\n"
"v_cvt_pk_bf16_f32 v176, v164, v166\n"
"v_cvt_pk_bf16_f32 v180, -v165, -v167\n"
"v_pk_mul_f32 v[172:173], v[168:169], v[158:159] op_sel:[1,1] op_sel_hi:[1,0]\n"
"v_pk_mul_f32 v[174:175], v[170:171], v[158:159] op_sel:[1,1] op_sel_hi:[1,0]\n"
"v_pk_fma_f32 v[164:165], v[168:169], v[158:159], v[172:173] op_sel_hi:[0,1,1] neg_lo:[0,0,1]\n"
"v_pk_fma_f32 v[166:167], v[170:171], v[158:159], v[174:175] op_sel_hi:[0,1,1] neg_lo:[0,0,1]\n"
"v_cvt_pk_bf16_f32 v177, v168, v170\n"
"v_cvt_pk_bf16_f32 v181, -v169, -v171\n"
"v_xor_b32 v188, 0x50, v189\n"
"ds_write_b128 v188, v[144:147]\n"
"v_pk_mul_f32 v[172:173], v[164:165], v[158:159] op_sel:[1,1] op_sel_hi:[1,0]\n"
"v_pk_mul_f32 v[174:175], v[166:167], v[158:159] op_sel:[1,1] op_sel_hi:[1,0]\n"
"v_pk_fma_f32 v[168:169], v[164:165], v[158:159], v[172:173] op_sel_hi:[0,1,1] neg_lo:[0,0,1]\n"
"v_pk_fma_f32 v[170:171], v[166:167], v[158:159], v[174:175] op_sel_hi:[0,1,1] neg_lo:[0,0,1]\n"
"v_cvt_pk_bf16_f32 v178, v164, v166\n"
"v_cvt_pk_bf16_f32 v182, -v165, -v167\n"
"v_pk_mul_f32 v[172:173], v[168:169], v[158:159] op_sel:[1,1] op_sel_hi:[1,0]\n"
"v_pk_mul_f32 v[174:175], v[170:171], v[158:159] op_sel:[1,1] op_sel_hi:[1,0]\n"
"v_pk_fma_f32 v[164:165], v[168:169], v[158:159], v[172:173] op_sel_hi:[0,1,1] neg_lo:[0,0,1]\n"
"v_pk_fma_f32 v[166:167], v[170:171], v[158:159], v[174:175] op_sel_hi:[0,1,1] neg_lo:[0,0,1]\n"
"v_cvt_pk_bf16_f32 v179, v168, v170\n"
"v_cvt_pk_bf16_f32 v183, -v169, -v171\n"
"v_xor_b32 v188, 32, v189\n"
"ds_write_b128 v188, v[176:179]\n"
"v_pk_mul_f32 v[172:173], v[164:165], v[158:159] op_sel:[1,1] op_sel_hi:[1,0]\n"
"v_pk_mul_f32 v[174:175], v[166:167], v[158:159] op_sel:[1,1] op_sel_hi:[1,0]\n"
"v_pk_fma_f32 v[168:169], v[164:165], v[158:159], v[172:173] op_sel_hi:[0,1,1] neg_lo:[0,0,1]\n"
"v_pk_fma_f32 v[170:171], v[166:167], v[158:159], v[174:175] op_sel_hi:[0,1,1] neg_lo:[0,0,1]\n"
"v_cvt_pk_bf16_f32 v176, v164, v166\n"
"v_cvt_pk_bf16_f32 v144, -v165, -v167\n"
"v_pk_mul_f32 v[172:173], v[168:169], v[158:159] op_sel:[1,1] op_sel_hi:[1,0]\n"
"v_pk_mul_f32 v[174:175], v[170:171], v[158:159] op_sel:[1,1] op_sel_hi:[1,0]\n"
"v_pk_fma_f32 v[164:165], v[168:169], v[158:159], v[172:173] op_sel_hi:[0,1,1] neg_lo:[0,0,1]\n"
"v_pk_fma_f32 v[166:167], v[170:171], v[158:159], v[174:175] op_sel_hi:[0,1,1] neg_lo:[0,0,1]\n"
"v_cvt_pk_bf16_f32 v177, v168, v170\n"
"v_cvt_pk_bf16_f32 v145, -v169, -v171\n"
"v_xor_b32 v188, 0x60, v189\n"
"ds_write_b128 v188, v[180:183]\n"
"v_pk_mul_f32 v[172:173], v[164:165], v[158:159] op_sel:[1,1] op_sel_hi:[1,0]\n"
"v_pk_mul_f32 v[174:175], v[166:167], v[158:159] op_sel:[1,1] op_sel_hi:[1,0]\n"
"v_pk_fma_f32 v[168:169], v[164:165], v[158:159], v[172:173] op_sel_hi:[0,1,1] neg_lo:[0,0,1]\n"
"v_pk_fma_f32 v[170:171], v[166:167], v[158:159], v[174:175] op_sel_hi:[0,1,1] neg_lo:[0,0,1]\n"
"v_cvt_pk_bf16_f32 v178, v164, v166\n"
"v_cvt_pk_bf16_f32 v146, -v165, -v167\n"
"v_cvt_pk_bf16_f32 v179, v168, v170\n"
"v_cvt_pk_bf16_f32 v147, -v169, -v171\n"
"v_xor_b32 v188, 48, v189\n"
"ds_write_b128 v188, v[176:179]\n"
"v_mul_f32 v172, 1.0, v156\n"
"v_mul_f32 v173, 1.0, v157\n"
"v_exp_f32 v172, v172\n"
"v_fract_f32 v173, v173\n"
"v_cos_f32 v158, v173\n"
"v_sin_f32 v159, v173\n"
"v_mul_f32 v158, v172, v158\n"
"v_mul_f32 v159, v172, v159\n"
"v_xor_b32 v188, 0x70, v189\n"
"ds_write_b128 v188, v[144:147]\n"
"ds_read_b64_tr_b16 v[96:97], v190\n"
"ds_read_b64_tr_b16 v[98:99], v191\n"
"ds_read_b64_tr_b16 v[100:101], v190 offset:1024\n"
"ds_read_b64_tr_b16 v[102:103], v191 offset:1024\n"
"ds_read_b64_tr_b16 v[104:105], v190 offset:2048\n"
"ds_read_b64_tr_b16 v[106:107], v191 offset:2048\n"
"ds_read_b64_tr_b16 v[108:109], v190 offset:3072\n"
"ds_read_b64_tr_b16 v[110:111], v191 offset:3072\n"
"ds_read_b64_tr_b16 v[112:113], v190 offset:4096\n"
"ds_read_b64_tr_b16 v[114:115], v191 offset:4096\n"
"ds_read_b64_tr_b16 v[116:117], v190 offset:5120\n"
"ds_read_b64_tr_b16 v[118:119], v191 offset:5120\n"
"ds_read_b64_tr_b16 v[120:121], v190 offset:6144\n"
"ds_read_b64_tr_b16 v[122:123], v191 offset:6144\n"
"ds_read_b64_tr_b16 v[124:125], v190 offset:7168\n"
"ds_read_b64_tr_b16 v[126:127], v191 offset:7168\n"
"v_mul_f32 v172, 0x43800000, v156\n"
"v_mul_f32 v173, 0x43800000, v157\n"
"v_exp_f32 v172, v172\n"
"v_fract_f32 v173, v173\n"
"v_cos_f32 v162, v173\n"
"v_sin_f32 v163, v173\n"
"v_mul_f32 v162, v172, v162\n"
"v_mul_f32 v163, v172, v163\n"
"v_pk_mul_f32 v[172:173], v[158:159], v[158:159] op_sel:[1,1] op_sel_hi:[1,0]\n"
"v_pk_mul_f32 v[174:175], v[162:163], v[162:163] op_sel:[1,1] op_sel_hi:[1,0]\n"
"v_pk_fma_f32 v[160:161], v[158:159], v[158:159], v[172:173] op_sel_hi:[0,1,1] neg_lo:[0,0,1]\n"
"v_pk_fma_f32 v[168:169], v[162:163], v[162:163], v[174:175] op_sel_hi:[0,1,1] neg_lo:[0,0,1]\n"
"v_cvt_pk_bf16_f32 v176, 1.0, v158\n"
"v_cvt_pk_bf16_f32 v180, 0, v159\n"
"v_pk_mul_f32 v[172:173], v[160:161], v[158:159] op_sel:[1,1] op_sel_hi:[1,0]\n"
"v_pk_mul_f32 v[174:175], v[160:161], v[160:161] op_sel:[1,1] op_sel_hi:[1,0]\n"
"v_pk_fma_f32 v[184:185], v[160:161], v[158:159], v[172:173] op_sel_hi:[0,1,1] neg_lo:[0,0,1]\n"
"v_pk_fma_f32 v[164:165], v[160:161], v[160:161], v[174:175] op_sel_hi:[0,1,1] neg_lo:[0,0,1]\n"
"v_cvt_pk_bf16_f32 v177, v160, v184\n"
"v_cvt_pk_bf16_f32 v181, v161, v185\n"
"v_pk_mul_f32 v[172:173], v[164:165], v[158:159] op_sel:[1,1] op_sel_hi:[1,0]\n"
"v_pk_mul_f32 v[174:175], v[164:165], v[160:161] op_sel:[1,1] op_sel_hi:[1,0]\n"
"v_pk_fma_f32 v[184:185], v[164:165], v[158:159], v[172:173] op_sel_hi:[0,1,1] neg_lo:[0,0,1]\n"
"v_pk_fma_f32 v[166:167], v[164:165], v[160:161], v[174:175] op_sel_hi:[0,1,1] neg_lo:[0,0,1]\n"
"v_cvt_pk_bf16_f32 v178, v164, v184\n"
"v_cvt_pk_bf16_f32 v182, v165, v185\n"
"v_pk_mul_f32 v[172:173], v[166:167], v[158:159] op_sel:[1,1] op_sel_hi:[1,0]\n"
"v_pk_mul_f32 v[174:175], v[162:163], v[160:161] op_sel:[1,1] op_sel_hi:[1,0]\n"
"v_pk_fma_f32 v[184:185], v[166:167], v[158:159], v[172:173] op_sel_hi:[0,1,1] neg_lo:[0,0,1]\n"
"v_pk_fma_f32 v[186:187], v[162:163], v[160:161], v[174:175] op_sel_hi:[0,1,1] neg_lo:[0,0,1]\n"
"v_cvt_pk_bf16_f32 v179, v166, v184\n"
"v_cvt_pk_bf16_f32 v183, v167, v185\n"
"ds_write_b128 v189, v[176:179]\n"
"v_cvt_pk_bf16_f32 v176, v162, v186\n"
"v_cvt_pk_bf16_f32 v144, v163, v187\n"
"v_pk_mul_f32 v[172:173], v[162:163], v[164:165] op_sel:[1,1] op_sel_hi:[1,0]\n"
"v_pk_mul_f32 v[174:175], v[162:163], v[166:167] op_sel:[1,1] op_sel_hi:[1,0]\n"
"v_pk_fma_f32 v[184:185], v[162:163], v[164:165], v[172:173] op_sel_hi:[0,1,1] neg_lo:[0,0,1]\n"
"v_pk_fma_f32 v[186:187], v[162:163], v[166:167], v[174:175] op_sel_hi:[0,1,1] neg_lo:[0,0,1]\n"
"v_cvt_pk_bf16_f32 v177, v184, v186\n"
"v_cvt_pk_bf16_f32 v145, v185, v187\n"
"s_setprio 3\n"
"buffer_load_dwordx2 v[234:235], v233, s[52:55], 0 offen sc1\n"
"global_load_dwordx4 v[204:207], v254, s[98:99]\n"
"global_load_dwordx4 v[208:211], v254, s[98:99] offset:1024\n"
"global_load_dwordx2 v[212:213], v255, s[98:99] offset:2048\n"
"s_setprio 0\n"
"v_xor_b32 v188, 64, v189\n"
"ds_write_b128 v188, v[180:183]\n"
"v_pk_mul_f32 v[172:173], v[168:169], v[164:165] op_sel:[1,1] op_sel_hi:[1,0]\n"
"v_pk_mul_f32 v[174:175], v[168:169], v[162:163] op_sel:[1,1] op_sel_hi:[1,0]\n"
"v_pk_fma_f32 v[184:185], v[168:169], v[164:165], v[172:173] op_sel_hi:[0,1,1] neg_lo:[0,0,1]\n"
"v_pk_fma_f32 v[170:171], v[168:169], v[162:163], v[174:175] op_sel_hi:[0,1,1] neg_lo:[0,0,1]\n"
"v_cvt_pk_bf16_f32 v178, v168, v184\n"
"v_cvt_pk_bf16_f32 v146, v169, v185\n"
"v_pk_mul_f32 v[172:173], v[170:171], v[164:165] op_sel:[1,1] op_sel_hi:[1,0]\n"
"v_pk_mul_f32 v[174:175], v[168:169], v[168:169] op_sel:[1,1] op_sel_hi:[1,0]\n"
"v_pk_fma_f32 v[184:185], v[170:171], v[164:165], v[172:173] op_sel_hi:[0,1,1] neg_lo:[0,0,1]\n"
"v_pk_fma_f32 v[186:187], v[168:169], v[168:169], v[174:175] op_sel_hi:[0,1,1] neg_lo:[0,0,1]\n"
"v_cvt_pk_bf16_f32 v179, v170, v184\n"
"v_cvt_pk_bf16_f32 v147, v171, v185\n"
"v_xor_b32 v188, 16, v189\n"
"ds_write_b128 v188, v[176:179]\n"
"v_pk_mul_f32 v[172:173], v[186:187], v[162:163] op_sel:[1,1] op_sel_hi:[1,0]\n"
"v_pk_mul_f32 v[174:175], v[186:187], v[168:169] op_sel:[1,1] op_sel_hi:[1,0]\n"
"v_pk_fma_f32 v[184:185], v[186:187], v[162:163], v[172:173] op_sel_hi:[0,1,1] neg_lo:[0,0,1]\n"
"v_pk_fma_f32 v[160:161], v[186:187], v[168:169], v[174:175] op_sel_hi:[0,1,1] neg_lo:[0,0,1]\n"
"v_cvt_pk_bf16_f32 v176, v186, v184\n"
"v_cvt_pk_bf16_f32 v180, v187, v185\n"
"v_xor_b32 v188, 0x50, v189\n"
"ds_write_b128 v188, v[144:147]\n"
"v_pk_mul_f32 v[172:173], v[186:187], v[170:171] op_sel:[1,1] op_sel_hi:[1,0]\n"
"v_pk_mul_f32 v[174:175], v[186:187], v[186:187] op_sel:[1,1] op_sel_hi:[1,0]\n"
"v_pk_fma_f32 v[184:185], v[186:187], v[170:171], v[172:173] op_sel_hi:[0,1,1] neg_lo:[0,0,1]\n"
"v_pk_fma_f32 v[164:165], v[186:187], v[186:187], v[174:175] op_sel_hi:[0,1,1] neg_lo:[0,0,1]\n"
"v_cvt_pk_bf16_f32 v177, v160, v184\n"
"v_cvt_pk_bf16_f32 v181, v161, v185\n"
"v_cvt_pk_bf16_f32 v178, v164, v164\n"
"v_cvt_pk_bf16_f32 v182, v165, v165\n"
"v_xor_b32 v188, 32, v189\n"
"ds_write_b128 v188, v[176:179]\n"
"v_xor_b32 v188, 0x60, v189\n"
"ds_write_b128 v188, v[180:183]\n"
"ds_read_b64_tr_b16 v[144:145], v190\n"
"ds_read_b64_tr_b16 v[146:147], v191\n"
"ds_read_b64_tr_b16 v[148:149], v190 offset:1024\n"
"ds_read_b64_tr_b16 v[150:151], v191 offset:1024\n"
"v_and_b32 v156, 0xffff0000, v152\n"
"v_and_b32 v157, 0xffff0000, v153\n"
"v_mul_f32 v156, 0x3fb8aa3b, v156\n"
"s_waitcnt lgkmcnt(2)\n"
"v_mfma_f32_32x32x16_bf16 v[128:143], v[96:99], v[144:147], 0\n"
"ds_read_b64_tr_b16 v[144:145], v190 offset:2048\n"
"ds_read_b64_tr_b16 v[146:147], v191 offset:2048\n"
"v_mul_f32 v157, v192, v157\n"
"v_exp_f32 v156, v156\n"
"v_mul_f32 v157, 0x3e22f983, v157\n"
"s_waitcnt lgkmcnt(2)\n"
"v_mfma_f32_32x32x16_bf16 v[128:143], v[100:103], v[148:151], v[128:143]\n"
"ds_read_b64_tr_b16 v[148:149], v190 offset:3072\n"
"ds_read_b64_tr_b16 v[150:151], v191 offset:3072\n"
"v_and_b32 v164, 0xffff0000, v154\n"
"v_cvt_pk_bf16_f32 v156, v156, v156\n"
"v_and_b32 v165, 0xffff0000, v155\n"
"s_waitcnt lgkmcnt(2)\n"
"v_mfma_f32_32x32x16_bf16 v[128:143], v[104:107], v[144:147], v[128:143]\n"
"ds_read_b64_tr_b16 v[144:145], v190 offset:4096\n"
"ds_read_b64_tr_b16 v[146:147], v191 offset:4096\n"
"v_and_b32 v156, 0xffff0000, v156\n"
"v_mul_f32 v164, v192, v164\n"
"v_mul_f32 v156, v192, v156\n"
"s_waitcnt lgkmcnt(2)\n"
"v_mfma_f32_32x32x16_bf16 v[128:143], v[108:111], v[148:151], v[128:143]\n"
"ds_read_b64_tr_b16 v[148:149], v190 offset:5120\n"
"ds_read_b64_tr_b16 v[150:151], v191 offset:5120\n"
"v_mul_f32 v165, v192, v165\n"
"v_mul_f32 v156, 0xbfb8aa3b, v156\n"
"v_add_f32 v164, v164, v164\n"
"s_waitcnt lgkmcnt(2)\n"
"v_mfma_f32_32x32x16_bf16 v[128:143], v[112:115], v[144:147], v[128:143]\n"
"ds_read_b64_tr_b16 v[144:145], v190 offset:6144\n"
"ds_read_b64_tr_b16 v[146:147], v191 offset:6144\n"
"v_add_f32 v165, v165, v165\n"
"v_mul_f32 v172, 0x41000000, v156\n"
"v_mul_f32 v173, 0x41000000, v157\n"
"s_waitcnt lgkmcnt(2)\n"
"v_mfma_f32_32x32x16_bf16 v[128:143], v[116:119], v[148:151], v[128:143]\n"
"ds_read_b64_tr_b16 v[148:149], v190 offset:7168\n"
"ds_read_b64_tr_b16 v[150:151], v191 offset:7168\n"
"v_exp_f32 v172, v172\n"
"v_fract_f32 v173, v173\n"
"v_cos_f32 v160, v173\n"
"s_waitcnt lgkmcnt(2)\n"
"v_mfma_f32_32x32x16_bf16 v[128:143], v[120:123], v[144:147], v[128:143]\n"
"v_sin_f32 v161, v173\n"
"v_mul_f32 v160, v172, v160\n"
"v_mul_f32 v161, v172, v161\n"
"s_waitcnt lgkmcnt(0)\n"
"v_mfma_f32_32x32x16_bf16 v[128:143], v[124:127], v[148:151], v[128:143]\n"
"v_mul_f32 v172, v165, v161\n"
"v_mul_f32 v174, v161, v161\n"
"v_mul_f32 v167, v165, v160\n"
"v_mul_f32 v159, v161, v160\n"
"v_fma_f32 v166, v164, v160, -v172\n"
"v_fma_f32 v158, v160, v160, -v174\n"
"v_fmac_f32 v167, v164, v161\n"
"v_fmac_f32 v159, v160, v161\n"
"v_xor_b32 v172, 0x80000000, v159\n"
"v_mul_f32 v168, v164, v158\n"
"v_mul_f32 v170, v166, v158\n"
"v_mul_f32 v169, v165, v158\n"
"v_mul_f32 v171, v167, v158\n"
"v_fmac_f32 v168, v165, v172\n"
"v_fmac_f32 v170, v167, v172\n"
"v_fmac_f32 v169, v164, v159\n"
"s_mul_i32 s75, s96, 0x1c00\n"
"s_mul_i32 s77, s97, 0x700\n"
"s_add_u32 s75, s75, s77\n"
"v_min_u32 v251, 13, v232\n"
"v_lshlrev_b32 v251, 7, v251\n"
"s_setprio 3\n"
"buffer_load_dword v220, v251, s[48:51], s75 offen\n"
"s_setprio 0\n"
"v_fmac_f32 v171, v166, v159\n"
"v_cvt_pk_bf16_f32 v176, v164, v166\n"
"v_cvt_pk_bf16_f32 v180, -v165, -v167\n"
"v_mul_f32 v164, v168, v158\n"
"v_mul_f32 v166, v170, v158\n"
"v_mul_f32 v165, v169, v158\n"
"v_mul_f32 v167, v171, v158\n"
"v_fmac_f32 v164, v169, v172\n"
"v_fmac_f32 v166, v171, v172\n"
"v_fmac_f32 v165, v168, v159\n"
"v_fmac_f32 v167, v170, v159\n"
"v_cvt_pk_bf16_f32 v177, v168, v170\n"
"v_cvt_pk_bf16_f32 v181, -v169, -v171\n"
"v_mul_f32 v168, v164, v158\n"
"v_mul_f32 v170, v166, v158\n"
"v_mul_f32 v169, v165, v158\n"
"v_mul_f32 v171, v167, v158\n"
"v_fmac_f32 v168, v165, v172\n"
"v_fmac_f32 v170, v167, v172\n"
"v_fmac_f32 v169, v164, v159\n"
"v_fmac_f32 v171, v166, v159\n"
"v_cvt_pk_bf16_f32 v178, v164, v166\n"
"v_cvt_pk_bf16_f32 v182, -v165, -v167\n"
"v_mul_f32 v164, v168, v158\n"
"v_mul_f32 v166, v170, v158\n"
"v_mul_f32 v165, v169, v158\n"
"v_mul_f32 v167, v171, v158\n"
"v_fmac_f32 v164, v169, v172\n"
"v_fmac_f32 v166, v171, v172\n"
"v_fmac_f32 v165, v168, v159\n"
"v_fmac_f32 v167, v170, v159\n"
"v_cvt_pk_bf16_f32 v179, v168, v170\n"
"v_cvt_pk_bf16_f32 v183, -v169, -v171\n"
"ds_write_b128 v189, v[176:179]\n"
"v_mul_f32 v168, v164, v158\n"
"v_mul_f32 v170, v166, v158\n"
"v_mul_f32 v169, v165, v158\n"
"v_mul_f32 v171, v167, v158\n"
"v_fmac_f32 v168, v165, v172\n"
"v_fmac_f32 v170, v167, v172\n"
"v_fmac_f32 v169, v164, v159\n"
"v_fmac_f32 v171, v166, v159\n"
"v_cvt_pk_bf16_f32 v176, v164, v166\n"
"v_cvt_pk_bf16_f32 v144, -v165, -v167\n"
"v_mul_f32 v164, v168, v158\n"
"v_mul_f32 v166, v170, v158\n"
"v_mul_f32 v165, v169, v158\n"
"v_mul_f32 v167, v171, v158\n"
"v_fmac_f32 v164, v169, v172\n"
"v_fmac_f32 v166, v171, v172\n"
"v_fmac_f32 v165, v168, v159\n"
"v_fmac_f32 v167, v170, v159\n"
"v_cvt_pk_bf16_f32 v177, v168, v170\n"
"v_cvt_pk_bf16_f32 v145, -v169, -v171\n"
"v_xor_b32 v188, 64, v189\n"
"ds_write_b128 v188, v[180:183]\n"
"v_mul_f32 v168, v164, v158\n"
"v_mul_f32 v170, v166, v158\n"
"v_mul_f32 v169, v165, v158\n"
"v_mul_f32 v171, v167, v158\n"
"v_fmac_f32 v168, v165, v172\n"
"v_fmac_f32 v170, v167, v172\n"
"v_fmac_f32 v169, v164, v159\n"
"v_fmac_f32 v171, v166, v159\n"
"v_cvt_pk_bf16_f32 v178, v164, v166\n"
"v_cvt_pk_bf16_f32 v146, -v165, -v167\n"
"v_mul_f32 v164, v168, v158\n"
"v_mul_f32 v166, v170, v158\n"
"v_mul_f32 v165, v169, v158\n"
"v_mul_f32 v167, v171, v158\n"
"v_fmac_f32 v164, v169, v172\n"
"v_fmac_f32 v166, v171, v172\n"
"v_fmac_f32 v165, v168, v159\n"
"v_fmac_f32 v167, v170, v159\n"
"v_cvt_pk_bf16_f32 v179, v168, v170\n"
"v_cvt_pk_bf16_f32 v147, -v169, -v171\n"
"v_xor_b32 v188, 16, v189\n"
"ds_write_b128 v188, v[176:179]\n"
"v_mul_f32 v168, v164, v158\n"
"v_mul_f32 v170, v166, v158\n"
"s_waitcnt vmcnt(4)\n"
"v_readfirstlane_b32 s60, v234\n"
"v_readfirstlane_b32 s61, v235\n"
"s_add_u32 s60, s60, 0x9E3779B9\n"
"s_addc_u32 s61, s61, 0x7F4A7C15\n"
"v_mov_b32 v238, s60\n"
"v_mov_b32 v239, s61\n"
"v_mov_b32 v251, 0x27e74\n"
"s_mov_b64 exec, 1\n"
"ds_add_u32 v251, v249\n"
"s_mov_b64 exec, -1\n"
"s_lshl_b32 s75, s66, 11\n"
"s_lshl_b32 s77, s71, 14\n"
"s_add_u32 s75, s75, s77\n"
"s_add_u32 s75, s75, 0xb03000\n"
"v_min_u32 v251, 15, v232\n"
"v_lshl_add_u32 v218, v251, 7, s75\n"
"v_mul_f32 v169, v165, v158\n"
"v_mul_f32 v171, v167, v158\n"
"v_fmac_f32 v168, v165, v172\n"
"v_fmac_f32 v170, v167, v172\n"
"v_fmac_f32 v169, v164, v159\n"
"v_fmac_f32 v171, v166, v159\n"
"v_cvt_pk_bf16_f32 v176, v164, v166\n"
"v_cvt_pk_bf16_f32 v180, -v165, -v167\n"
"v_mul_f32 v164, v168, v158\n"
"v_mul_f32 v166, v170, v158\n"
"v_mul_f32 v165, v169, v158\n"
"v_mul_f32 v167, v171, v158\n"
"v_fmac_f32 v164, v169, v172\n"
"v_fmac_f32 v166, v171, v172\n"
"v_fmac_f32 v165, v168, v159\n"
"v_fmac_f32 v167, v170, v159\n"
"v_cvt_pk_bf16_f32 v177, v168, v170\n"
"v_cvt_pk_bf16_f32 v181, -v169, -v171\n"
"v_xor_b32 v188, 0x50, v189\n"
"ds_write_b128 v188, v[144:147]\n"
"v_mul_f32 v168, v164, v158\n"
"v_mul_f32 v170, v166, v158\n"
"v_mul_f32 v169, v165, v158\n"
"v_mul_f32 v171, v167, v158\n"
"v_fmac_f32 v168, v165, v172\n"
"v_fmac_f32 v170, v167, v172\n"
"v_fmac_f32 v169, v164, v159\n"
"v_fmac_f32 v171, v166, v159\n"
"v_cvt_pk_bf16_f32 v178, v164, v166\n"
"v_cvt_pk_bf16_f32 v182, -v165, -v167\n"
"v_mul_f32 v164, v168, v158\n"
"v_mul_f32 v166, v170, v158\n"
"v_mul_f32 v165, v169, v158\n"
"v_mul_f32 v167, v171, v158\n"
"v_fmac_f32 v164, v169, v172\n"
"v_fmac_f32 v166, v171, v172\n"
"v_fmac_f32 v165, v168, v159\n"
"v_fmac_f32 v167, v170, v159\n"
"v_cvt_pk_bf16_f32 v179, v168, v170\n"
"v_cvt_pk_bf16_f32 v183, -v169, -v171\n"
"v_xor_b32 v188, 32, v189\n"
"ds_write_b128 v188, v[176:179]\n"
"v_mul_f32 v168, v164, v158\n"
"v_mul_f32 v170, v166, v158\n"
"v_mul_f32 v169, v165, v158\n"
"v_mul_f32 v171, v167, v158\n"
"v_fmac_f32 v168, v165, v172\n"
"v_fmac_f32 v170, v167, v172\n"
"v_fmac_f32 v169, v164, v159\n"
"v_fmac_f32 v171, v166, v159\n"
"v_cvt_pk_bf16_f32 v176, v164, v166\n"
"v_cvt_pk_bf16_f32 v144, -v165, -v167\n"
"v_mul_f32 v164, v168, v158\n"
"v_mul_f32 v166, v170, v158\n"
"v_mul_f32 v165, v169, v158\n"
"v_mul_f32 v167, v171, v158\n"
"v_fmac_f32 v164, v169, v172\n"
"v_fmac_f32 v166, v171, v172\n"
"v_fmac_f32 v165, v168, v159\n"
"v_fmac_f32 v167, v170, v159\n"
"v_cvt_pk_bf16_f32 v177, v168, v170\n"
"v_cvt_pk_bf16_f32 v145, -v169, -v171\n"
"v_xor_b32 v188, 0x60, v189\n"
"ds_write_b128 v188, v[180:183]\n"
"v_mul_f32 v168, v164, v158\n"
"v_mul_f32 v170, v166, v158\n"
"v_mul_f32 v169, v165, v158\n"
"v_mul_f32 v171, v167, v158\n"
"v_fmac_f32 v168, v165, v172\n"
"v_fmac_f32 v170, v167, v172\n"
"v_fmac_f32 v169, v164, v159\n"
"v_fmac_f32 v171, v166, v159\n"
"v_cvt_pk_bf16_f32 v178, v164, v166\n"
"v_cvt_pk_bf16_f32 v146, -v165, -v167\n"
"v_cvt_pk_bf16_f32 v179, v168, v170\n"
"v_cvt_pk_bf16_f32 v147, -v169, -v171\n"
"v_xor_b32 v188, 48, v189\n"
"ds_write_b128 v188, v[176:179]\n"
"v_mul_f32 v172, 1.0, v156\n"
"v_mul_f32 v173, 1.0, v157\n"
"v_exp_f32 v172, v172\n"
"v_fract_f32 v173, v173\n"
"v_cos_f32 v158, v173\n"
"v_sin_f32 v159, v173\n"
"v_mul_f32 v158, v172, v158\n"
"v_mul_f32 v159, v172, v159\n"
"v_xor_b32 v188, 0x70, v189\n"
"ds_write_b128 v188, v[144:147]\n"
"ds_read_b64_tr_b16 v[96:97], v190\n"
"ds_read_b64_tr_b16 v[98:99], v191\n"
"ds_read_b64_tr_b16 v[100:101], v190 offset:1024\n"
"ds_read_b64_tr_b16 v[102:103], v191 offset:1024\n"
"ds_read_b64_tr_b16 v[104:105], v190 offset:2048\n"
"ds_read_b64_tr_b16 v[106:107], v191 offset:2048\n"
"ds_read_b64_tr_b16 v[108:109], v190 offset:3072\n"
"ds_read_b64_tr_b16 v[110:111], v191 offset:3072\n"
"ds_read_b64_tr_b16 v[112:113], v190 offset:4096\n"
"ds_read_b64_tr_b16 v[114:115], v191 offset:4096\n"
"ds_read_b64_tr_b16 v[116:117], v190 offset:5120\n"
"ds_read_b64_tr_b16 v[118:119], v191 offset:5120\n"
"ds_read_b64_tr_b16 v[120:121], v190 offset:6144\n"
"ds_read_b64_tr_b16 v[122:123], v191 offset:6144\n"
"ds_read_b64_tr_b16 v[124:125], v190 offset:7168\n"
"ds_read_b64_tr_b16 v[126:127], v191 offset:7168\n"
"v_mul_f32 v172, 0x43800000, v156\n"
"v_mul_f32 v173, 0x43800000, v157\n"
"v_exp_f32 v172, v172\n"
"v_fract_f32 v173, v173\n"
"v_cos_f32 v162, v173\n"
"v_sin_f32 v163, v173\n"
"v_mul_f32 v162, v172, v162\n"
"v_mul_f32 v163, v172, v163\n"
"v_mul_f32 v172, v159, v159\n"
"v_mul_f32 v174, v163, v163\n"
"v_mul_f32 v161, v159, v158\n"
"v_mul_f32 v169, v163, v162\n"
"v_fma_f32 v160, v158, v158, -v172\n"
"v_fma_f32 v168, v162, v162, -v174\n"
"v_fmac_f32 v161, v158, v159\n"
"v_fmac_f32 v169, v162, v163\n"
"v_cvt_pk_bf16_f32 v176, 1.0, v158\n"
"v_cvt_pk_bf16_f32 v180, 0, v159\n"
"v_mul_f32 v172, v161, v159\n"
"v_mul_f32 v174, v161, v161\n"
"v_mul_f32 v185, v161, v158\n"
"v_mul_f32 v165, v161, v160\n"
"v_fma_f32 v184, v160, v158, -v172\n"
"v_fma_f32 v164, v160, v160, -v174\n"
"v_fmac_f32 v185, v160, v159\n"
"v_fmac_f32 v165, v160, v161\n"
"v_cvt_pk_bf16_f32 v177, v160, v184\n"
"v_cvt_pk_bf16_f32 v181, v161, v185\n"
"v_mul_f32 v172, v165, v159\n"
"v_mul_f32 v174, v165, v161\n"
"v_mul_f32 v185, v165, v158\n"
"v_mul_f32 v167, v165, v160\n"
"v_fma_f32 v184, v164, v158, -v172\n"
"v_fma_f32 v166, v164, v160, -v174\n"
"v_fmac_f32 v185, v164, v159\n"
"v_fmac_f32 v167, v164, v161\n"
"v_cvt_pk_bf16_f32 v178, v164, v184\n"
"v_cvt_pk_bf16_f32 v182, v165, v185\n"
"v_mul_f32 v172, v167, v159\n"
"v_mul_f32 v174, v163, v161\n"
"v_mul_f32 v185, v167, v158\n"
"v_mul_f32 v187, v163, v160\n"
"v_fma_f32 v184, v166, v158, -v172\n"
"v_fma_f32 v186, v162, v160, -v174\n"
"v_fmac_f32 v185, v166, v159\n"
"v_fmac_f32 v187, v162, v161\n"
"v_cvt_pk_bf16_f32 v179, v166, v184\n"
"v_cvt_pk_bf16_f32 v183, v167, v185\n"
"ds_write_b128 v189, v[176:179]\n"
"v_cvt_pk_bf16_f32 v176, v162, v186\n"
"v_cvt_pk_bf16_f32 v144, v163, v187\n"
"v_mul_f32 v172, v163, v165\n"
"v_mul_f32 v174, v163, v167\n"
"v_mul_f32 v185, v163, v164\n"
"v_mul_f32 v187, v163, v166\n"
"v_fma_f32 v184, v162, v164, -v172\n"
"v_fma_f32 v186, v162, v166, -v174\n"
"v_fmac_f32 v185, v162, v165\n"
"v_fmac_f32 v187, v162, v167\n"
"v_cvt_pk_bf16_f32 v177, v184, v186\n"
"v_cvt_pk_bf16_f32 v145, v185, v187\n"
"v_xor_b32 v188, 64, v189\n"
"ds_write_b128 v188, v[180:183]\n"
"v_mul_f32 v172, v169, v165\n"
"v_mul_f32 v174, v169, v163\n"
"v_mul_f32 v185, v169, v164\n"
"v_mul_f32 v171, v169, v162\n"
"v_fma_f32 v184, v168, v164, -v172\n"
"v_fma_f32 v170, v168, v162, -v174\n"
"v_fmac_f32 v185, v168, v165\n"
"v_fmac_f32 v171, v168, v163\n"
"v_cvt_pk_bf16_f32 v178, v168, v184\n"
"v_cvt_pk_bf16_f32 v146, v169, v185\n"
"v_mul_f32 v172, v171, v165\n"
"v_mul_f32 v174, v169, v169\n"
"v_mul_f32 v185, v171, v164\n"
"v_mul_f32 v187, v169, v168\n"
"v_fma_f32 v184, v170, v164, -v172\n"
"v_fma_f32 v186, v168, v168, -v174\n"
"v_fmac_f32 v185, v170, v165\n"
"v_fmac_f32 v187, v168, v169\n"
"v_cvt_pk_bf16_f32 v179, v170, v184\n"
"v_cvt_pk_bf16_f32 v147, v171, v185\n"
"v_xor_b32 v188, 16, v189\n"
"ds_write_b128 v188, v[176:179]\n"
"v_mul_f32 v172, v187, v163\n"
"v_mul_f32 v174, v187, v169\n"
"v_mul_f32 v185, v187, v162\n"
"v_mul_f32 v161, v187, v168\n"
"v_fma_f32 v184, v186, v162, -v172\n"
"v_fma_f32 v160, v186, v168, -v174\n"
"v_fmac_f32 v185, v186, v163\n"
"v_fmac_f32 v161, v186, v169\n"
"v_cvt_pk_bf16_f32 v176, v186, v184\n"
"v_cvt_pk_bf16_f32 v180, v187, v185\n"
"v_xor_b32 v188, 0x50, v189\n"
"ds_write_b128 v188, v[144:147]\n"
"v_mul_f32 v172, v187, v171\n"
"v_mul_f32 v174, v187, v187\n"
"v_mul_f32 v185, v187, v170\n"
"v_mul_f32 v165, v187, v186\n"
"v_fma_f32 v184, v186, v170, -v172\n"
"v_fma_f32 v164, v186, v186, -v174\n"
"v_fmac_f32 v185, v186, v171\n"
"v_fmac_f32 v165, v186, v187\n"
"v_cvt_pk_bf16_f32 v177, v160, v184\n"
"v_cvt_pk_bf16_f32 v181, v161, v185\n"
"v_cvt_pk_bf16_f32 v178, v164, v164\n"
"v_cvt_pk_bf16_f32 v182, v165, v165\n"
"v_xor_b32 v188, 32, v189\n"
"ds_write_b128 v188, v[176:179]\n"
"v_xor_b32 v188, 0x60, v189\n"
"ds_write_b128 v188, v[180:183]\n"
"ds_read_b64_tr_b16 v[144:145], v190\n"
"ds_read_b64_tr_b16 v[146:147], v191\n"
"ds_read_b64_tr_b16 v[148:149], v190 offset:1024\n"
"ds_read_b64_tr_b16 v[150:151], v191 offset:1024\n"
"s_waitcnt lgkmcnt(2)\n"
"v_mfma_f32_32x32x16_bf16 v[128:143], v[96:99], v[144:147], v[128:143]\n"
"ds_read_b64_tr_b16 v[144:145], v190 offset:2048\n"
"ds_read_b64_tr_b16 v[146:147], v191 offset:2048\n"
"s_waitcnt lgkmcnt(2)\n"
"v_mfma_f32_32x32x16_bf16 v[128:143], v[100:103], v[148:151], v[128:143]\n"
"ds_read_b64_tr_b16 v[148:149], v190 offset:3072\n"
"ds_read_b64_tr_b16 v[150:151], v191 offset:3072\n"
"s_waitcnt lgkmcnt(2)\n"
"v_mfma_f32_32x32x16_bf16 v[128:143], v[104:107], v[144:147], v[128:143]\n"
"ds_read_b64_tr_b16 v[144:145], v190 offset:4096\n"
"ds_read_b64_tr_b16 v[146:147], v191 offset:4096\n"
"v_lshlrev_b32 v251, 4, v232\n"
"v_add_u32 v252, s91, v251\n"
"s_waitcnt lgkmcnt(2)\n"
"v_mfma_f32_32x32x16_bf16 v[128:143], v[108:111], v[148:151], v[128:143]\n"
"ds_read_b64_tr_b16 v[148:149], v190 offset:5120\n"
"ds_read_b64_tr_b16 v[150:151], v191 offset:5120\n"
"s_waitcnt lgkmcnt(2)\n"
"v_mfma_f32_32x32x16_bf16 v[128:143], v[112:115], v[144:147], v[128:143]\n"
"ds_read_b64_tr_b16 v[144:145], v190 offset:6144\n"
"ds_read_b64_tr_b16 v[146:147], v191 offset:6144\n"
"s_waitcnt lgkmcnt(2)\n"
"v_mfma_f32_32x32x16_bf16 v[128:143], v[116:119], v[148:151], v[128:143]\n"
"ds_read_b64_tr_b16 v[148:149], v190 offset:7168\n"
"ds_read_b64_tr_b16 v[150:151], v191 offset:7168\n"
"s_waitcnt lgkmcnt(2)\n"
"v_mfma_f32_32x32x16_bf16 v[128:143], v[120:123], v[144:147], v[128:143]\n"
"s_waitcnt lgkmcnt(0)\n"
"v_mfma_f32_32x32x16_bf16 v[128:143], v[124:127], v[148:151], v[128:143]\n"
"s_nop 11\n"
"ds_write_b128 v252, v[128:131]\n"
"ds_write_b128 v252, v[132:135] offset:1024\n"
"ds_write_b128 v252, v[136:139] offset:2048\n"
"ds_write_b128 v252, v[140:143] offset:3072\n"
"s_waitcnt lgkmcnt(0)\n"
"s_mov_b64 exec, 1\n"
"ds_add_u32 v248, v249\n"
"s_mov_b64 exec, -1\n"
"s_mul_i32 s75, s70, 0x1080\n"
"s_lshl_b32 s77, s66, 11\n"
"s_add_u32 s75, s75, s77\n"
"s_add_u32 s75, s75, 0x20000\n"
"v_lshl_add_u32 v230, v232, 4, s75\n"
"ds_read_b32 v250, v248\n"
"s_xor_b32 s76, s97, 1\n"
"s_mul_i32 s75, s76, 0x2200\n"
"s_lshl_b32 s77, s66, 11\n"
"s_add_u32 s75, s75, s77\n"
"s_add_u32 s75, s75, 0x18000\n"
"v_lshlrev_b32 v251, 4, v232\n"
"v_add_u32 v253, s75, v251\n"
"s_mul_i32 s75, s97, 0x2200\n"
"s_lshl_b32 s77, s66, 11\n"
"s_add_u32 s75, s75, s77\n"
"s_add_u32 s75, s75, 0x18000\n"
"v_add_u32 v252, s75, v251\n"
"s_mul_i32 s92, s67, 0xb10\n"
"s_add_u32 s92, s92, 0x26800\n"
"s_waitcnt lgkmcnt(0)\n"
"v_cmp_gt_u32 vcc, 2, v250\n"
"s_cbranch_vccnz .Lslow_c16\n"
".Lret_c16:\n"
"ds_read_b128 v[128:131], v252\n"
"ds_read_b128 v[132:135], v252 offset:1024\n"
"ds_read_b128 v[136:139], v253\n"
"ds_read_b128 v[140:143], v253 offset:1024\n"
"s_lshl_b32 s75, s66, 6\n"
"s_add_u32 s75, s75, s92\n"
"v_and_b32 v251, 31, v232\n"
"v_min_u32 v251, 21, v251\n"
"v_lshlrev_b32 v251, 7, v251\n"
"v_lshrrev_b32 v252, 5, v232\n"
"v_lshl_add_u32 v251, v252, 4, v251\n"
"v_add_u32 v251, s75, v251\n"
"v_cmp_eq_u32 vcc, 0, v232\n"
"v_mov_b32 v252, s89\n"
"s_nop 0\n"
"v_cndmask_b32 v252, 0, v252, vcc\n"
"v_mov_b32 v253, s92\n"
"s_waitcnt lgkmcnt(0)\n"
"v_add_f32 v128, v128, v136\n"
"v_add_f32 v129, v129, v137\n"
"v_add_f32 v130, v130, v138\n"
"v_add_f32 v131, v131, v139\n"
"v_add_f32 v132, v132, v140\n"
"v_add_f32 v133, v133, v141\n"
"v_add_f32 v134, v134, v142\n"
"v_add_f32 v135, v135, v143\n"
"v_add_f32 v128, v128, v252\n"
"ds_write_b128 v251, v[128:131]\n"
"ds_write_b128 v251, v[132:135] offset:32\n"
"ds_write_b32 v253, v233 offset:2816\n"
"s_waitcnt lgkmcnt(0)\n"
"s_mov_b64 exec, 1\n"
"ds_add_u32 v248, v249\n"
"s_mov_b64 exec, -1\n"
"ds_read_b32 v250, v248\n"
"s_waitcnt vmcnt(3)\n"
"v_add_u32_sdwa v160, v253, v204 dst_sel:DWORD dst_unused:UNUSED_PAD src0_sel:DWORD src1_sel:WORD_0\n"
"v_add_u32_sdwa v161, v253, v204 dst_sel:DWORD dst_unused:UNUSED_PAD src0_sel:DWORD src1_sel:WORD_1\n"
"v_add_u32_sdwa v162, v253, v205 dst_sel:DWORD dst_unused:UNUSED_PAD src0_sel:DWORD src1_sel:WORD_0\n"
"v_add_u32_sdwa v163, v253, v205 dst_sel:DWORD dst_unused:UNUSED_PAD src0_sel:DWORD src1_sel:WORD_1\n"
"v_add_u32_sdwa v164, v253, v206 dst_sel:DWORD dst_unused:UNUSED_PAD src0_sel:DWORD src1_sel:WORD_0\n"
"v_add_u32_sdwa v165, v253, v206 dst_sel:DWORD dst_unused:UNUSED_PAD src0_sel:DWORD src1_sel:WORD_1\n"
"v_add_u32_sdwa v166, v253, v207 dst_sel:DWORD dst_unused:UNUSED_PAD src0_sel:DWORD src1_sel:WORD_0\n"
"v_add_u32_sdwa v167, v253, v207 dst_sel:DWORD dst_unused:UNUSED_PAD src0_sel:DWORD src1_sel:WORD_1\n"
"s_setprio 3\n"
"buffer_load_dwordx2 v[216:217], v218, s[52:55], 0 offen sc1\n"
"s_setprio 0\n"
"s_waitcnt vmcnt(3)\n"
"v_add_u32_sdwa v168, v253, v208 dst_sel:DWORD dst_unused:UNUSED_PAD src0_sel:DWORD src1_sel:WORD_0\n"
"v_add_u32_sdwa v169, v253, v208 dst_sel:DWORD dst_unused:UNUSED_PAD src0_sel:DWORD src1_sel:WORD_1\n"
"v_add_u32_sdwa v170, v253, v209 dst_sel:DWORD dst_unused:UNUSED_PAD src0_sel:DWORD src1_sel:WORD_0\n"
"v_add_u32_sdwa v171, v253, v209 dst_sel:DWORD dst_unused:UNUSED_PAD src0_sel:DWORD src1_sel:WORD_1\n"
"v_add_u32_sdwa v172, v253, v210 dst_sel:DWORD dst_unused:UNUSED_PAD src0_sel:DWORD src1_sel:WORD_0\n"
"v_add_u32_sdwa v173, v253, v210 dst_sel:DWORD dst_unused:UNUSED_PAD src0_sel:DWORD src1_sel:WORD_1\n"
"v_add_u32_sdwa v174, v253, v211 dst_sel:DWORD dst_unused:UNUSED_PAD src0_sel:DWORD src1_sel:WORD_0\n"
"v_add_u32_sdwa v175, v253, v211 dst_sel:DWORD dst_unused:UNUSED_PAD src0_sel:DWORD src1_sel:WORD_1\n"
"s_waitcnt vmcnt(2)\n"
"v_add_u32_sdwa v176, v253, v212 dst_sel:DWORD dst_unused:UNUSED_PAD src0_sel:DWORD src1_sel:WORD_0\n"
"v_add_u32_sdwa v177, v253, v212 dst_sel:DWORD dst_unused:UNUSED_PAD src0_sel:DWORD src1_sel:WORD_1\n"
"v_add_u32_sdwa v178, v253, v213 dst_sel:DWORD dst_unused:UNUSED_PAD src0_sel:DWORD src1_sel:WORD_0\n"
"s_waitcnt lgkmcnt(0)\n"
"v_cmp_gt_u32 vcc, 4, v250\n"
"s_cbranch_vccnz .Lslow_c17\n"
".Lret_c17:\n"
"ds_read_b32 v160, v160\n"
"ds_read_b32 v161, v161\n"
"ds_read_b32 v162, v162\n"
"ds_read_b32 v163, v163\n"
"ds_read_b32 v164, v164\n"
"ds_read_b32 v165, v165\n"
"ds_read_b32 v166, v166\n"
"ds_read_b32 v167, v167\n"
"ds_read_b32 v168, v168\n"
"ds_read_b32 v169, v169\n"
"ds_read_b32 v170, v170\n"
"ds_read_b32 v171, v171\n"
"ds_read_b32 v172, v172\n"
"ds_read_b32 v173, v173\n"
"ds_read_b32 v174, v174\n"
"ds_read_b32 v175, v175\n"
"ds_read_b32 v176, v176\n"
"ds_read_b32 v177, v177\n"
"ds_read_b32 v178, v178\n"
"s_mul_i32 s75, s67, 0x4400\n"
"s_add_u32 s75, s75, 0x18000\n"
"s_lshl_b32 s76, s66, 11\n"
"s_add_u32 s76, s76, s75\n"
"v_lshlrev_b32 v251, 5, v232\n"
"v_add_u32 v251, s76, v251\n"
"v_bfe_u32 v252, v232, 1, 2\n"
"v_mul_u32_u24 v252, 0x1040, v252\n"
"v_and_b32 v253, 1, v232\n"
"s_waitcnt vmcnt(0)\n"
"v_cmp_ne_u64 vcc, s[60:61], v[216:217]\n"
"s_cbranch_vccnz .Lslow_g18\n"
".Lret_g18:\n"
"s_setprio 3\n"
"buffer_load_dwordx4 v[196:199], v230, s[52:55], 0 offen sc1\n"
"buffer_load_dwordx4 v[200:203], v230, s[52:55], 0 offen offset:1024 sc1\n"
"s_setprio 0\n"
"v_lshl_add_u32 v252, v253, 4, v252\n"
"v_add_u32 v252, s75, v252\n"
"s_waitcnt lgkmcnt(0)\n"
"v_cvt_pk_bf16_f32 v128, v160, v161\n"
"v_cvt_pk_bf16_f32 v129, v162, v163\n"
"v_cvt_pk_bf16_f32 v130, v164, v165\n"
"v_cvt_pk_bf16_f32 v131, v166, v167\n"
"ds_write_b128 v251, v[128:131]\n"
"v_cvt_pk_bf16_f32 v132, v168, v169\n"
"v_cvt_pk_bf16_f32 v133, v170, v171\n"
"v_cvt_pk_bf16_f32 v134, v172, v173\n"
"v_cvt_pk_bf16_f32 v135, v174, v175\n"
"ds_write_b128 v251, v[132:135] offset:16\n"
"v_cvt_pk_bf16_f32 v136, v161, v162\n"
"v_cvt_pk_bf16_f32 v137, v163, v164\n"
"v_cvt_pk_bf16_f32 v138, v165, v166\n"
"v_cvt_pk_bf16_f32 v139, v167, v168\n"
"ds_write_b128 v251, v[136:139] offset:4160\n"
"v_cvt_pk_bf16_f32 v140, v169, v170\n"
"v_cvt_pk_bf16_f32 v141, v171, v172\n"
"v_cvt_pk_bf16_f32 v142, v173, v174\n"
"v_cvt_pk_bf16_f32 v143, v175, v176\n"
"ds_write_b128 v251, v[140:143] offset:4176\n"
"v_cvt_pk_bf16_f32 v128, v162, v163\n"
"v_cvt_pk_bf16_f32 v129, v164, v165\n"
"v_cvt_pk_bf16_f32 v130, v166, v167\n"
"v_cvt_pk_bf16_f32 v131, v168, v169\n"
"ds_write_b128 v251, v[128:131] offset:8320\n"
"v_cvt_pk_bf16_f32 v132, v170, v171\n"
"v_cvt_pk_bf16_f32 v133, v172, v173\n"
"v_cvt_pk_bf16_f32 v134, v174, v175\n"
"v_cvt_pk_bf16_f32 v135, v176, v177\n"
"ds_write_b128 v251, v[132:135] offset:8336\n"
"v_cvt_pk_bf16_f32 v136, v163, v164\n"
"v_cvt_pk_bf16_f32 v137, v165, v166\n"
"v_cvt_pk_bf16_f32 v138, v167, v168\n"
"v_cvt_pk_bf16_f32 v139, v169, v170\n"
"ds_write_b128 v251, v[136:139] offset:12480\n"
"v_cvt_pk_bf16_f32 v140, v171, v172\n"
"v_cvt_pk_bf16_f32 v141, v173, v174\n"
"v_cvt_pk_bf16_f32 v142, v175, v176\n"
"v_cvt_pk_bf16_f32 v143, v177, v178\n"
"ds_write_b128 v251, v[140:143] offset:12496\n"
"v_mov_b32 v144, 0\n"
"v_mov_b32 v145, 0\n"
"v_mov_b32 v146, 0\n"
"v_mov_b32 v147, 0\n"
"ds_write_b128 v252, v[144:147] offset:4096\n"
"s_mul_i32 s92, s67, 0xffffc000\n"
"s_add_u32 s92, s92, 0x24800\n"
"v_bfe_u32 v185, v232, 2, 2\n"
"v_and_b32 v186, 3, v232\n"
"s_mul_i32 s75, s67, 0x4400\n"
"s_mul_i32 s77, s66, 0xffffff00\n"
"s_add_u32 s75, s75, s77\n"
"s_add_u32 s75, s75, 0x1b1d8\n"
"v_lshrrev_b32 v252, 4, v232\n"
"v_mul_u32_u24 v251, 0x1040, v186\n"
"v_lshl_add_u32 v251, v185, 3, v251\n"
"v_lshl_add_u32 v253, v252, 4, s75\n"
"v_sub_u32 v227, v253, v251\n"
"s_mul_i32 s75, s66, 0xffffff00\n"
"s_lshl_b32 s77, s92, 0\n"
"s_add_u32 s75, s75, s77\n"
"s_add_u32 s75, s75, 0x100\n"
"v_and_b32 v251, 15, v232\n"
"v_lshl_add_u32 v253, v252, 4, s75\n"
"v_lshl_add_u32 v228, v251, 5, v253\n"
"v_min_u32 v251, 29, v232\n"
"v_lshl_add_u32 v251, v251, 4, s92\n"
"s_lshl_b32 s76, s66, 11\n"
"s_add_u32 s75, s92, s76\n"
"v_lshl_add_u32 v252, v232, 4, s75\n"
"ds_write_b128 v251, v[144:147]\n"
"s_mul_i32 s75, s67, 0x4400\n"
"s_lshl_b32 s77, s66, 13\n"
"s_add_u32 s75, s75, s77\n"
"s_add_u32 s75, s75, 0x18000\n"
"v_and_b32 v251, 15, v232\n"
"v_lshrrev_b32 v187, 4, v232\n"
"v_bfe_u32 v253, v232, 1, 2\n"
"v_xor_b32 v187, v187, v253\n"
"v_lshlrev_b32 v187, 4, v187\n"
"v_lshl_add_u32 v251, v251, 6, v187\n"
"v_add_u32 v224, s75, v251\n"
"v_bfe_u32 v251, v232, 1, 4\n"
"v_and_b32 v187, 1, v232\n"
"v_lshlrev_b32 v187, 1, v187\n"
"v_bfe_u32 v253, v232, 2, 2\n"
"v_xor_b32 v187, v187, v253\n"
"v_lshlrev_b32 v187, 4, v187\n"
"v_lshl_add_u32 v251, v251, 6, v187\n"
"v_lshrrev_b32 v187, 5, v232\n"
"v_lshl_add_u32 v251, v187, 10, v251\n"
"s_mul_i32 s75, s67, 0x4400\n"
"s_lshl_b32 s77, s66, 12\n"
"s_add_u32 s75, s75, s77\n"
"s_add_u32 s75, s75, 0x18000\n"
"v_add_u32 v225, s75, v251\n"
"v_xor_b32 v226, 16, v225\n"
"v_add_u32 v229, 0x2000, v225\n"
"v_add_u32 v231, 0x2000, v226\n"
"s_lshl_b32 s93, s96, 7\n"
"s_lshl_b32 s77, s66, 4\n"
"s_add_u32 s93, s93, s77\n"
"s_lshl_b32 s77, s67, 3\n"
"s_add_u32 s93, s93, s77\n"
"s_add_u32 s93, s93, 0xb43000\n"
"s_waitcnt vmcnt(1)\n"
"ds_write_b128 v252, v[196:199] offset:480\n"
"s_waitcnt vmcnt(0)\n"
"ds_write_b128 v252, v[200:203] offset:1504\n"
"s_waitcnt lgkmcnt(0)\n"
"s_mov_b64 exec, 1\n"
"ds_add_u32 v248, v249\n"
"s_mov_b64 exec, -1\n"
"ds_read_b32 v250, v248\n"
"s_mov_b32 s92, 0x1e0000\n"
"s_waitcnt lgkmcnt(0)\n"
"v_cmp_gt_u32 vcc, 6, v250\n"
"s_cbranch_vccnz .Lslow_c19\n"
".Lret_c19:\n"
"s_setprio 0\n"
"ds_read_b64 v[128:129], v227 offset:192\n"
"ds_read_b64 v[130:131], v227 offset:200\n"
"ds_read_b128 v[132:135], v228 offset:192\n"
"ds_read_b64 v[136:137], v227 offset:704\n"
"ds_read_b64 v[138:139], v227 offset:712\n"
"ds_read_b128 v[140:143], v228 offset:704\n"
"ds_read_b64 v[144:145], v227 offset:1216\n"
"ds_read_b64 v[146:147], v227 offset:1224\n"
"ds_read_b128 v[148:151], v228 offset:1216\n"
"ds_read_b64 v[152:153], v227 offset:1728\n"
"ds_read_b64 v[154:155], v227 offset:1736\n"
"s_waitcnt lgkmcnt(8)\n"
"v_mfma_f32_16x16x32_bf16 v[124:127], v[128:131], v[132:135], 0\n"
"ds_read_b128 v[156:159], v228 offset:1728\n"
"ds_read_b64 v[160:161], v227 offset:2240\n"
"s_waitcnt lgkmcnt(8)\n"
"v_mfma_f32_16x16x32_bf16 v[120:123], v[136:139], v[132:135], 0\n"
"ds_read_b64 v[162:163], v227 offset:2248\n"
"ds_read_b128 v[164:167], v228 offset:2240\n"
"s_waitcnt lgkmcnt(9)\n"
"v_mfma_f32_16x16x32_bf16 v[124:127], v[136:139], v[140:143], v[124:127]\n"
"ds_read_b64 v[168:169], v227 offset:2752\n"
"ds_read_b64 v[170:171], v227 offset:2760\n"
"s_waitcnt lgkmcnt(8)\n"
"v_mfma_f32_16x16x32_bf16 v[116:119], v[144:147], v[132:135], 0\n"
"ds_read_b128 v[172:175], v228 offset:2752\n"
"ds_read_b64 v[176:177], v227 offset:3264\n"
"v_mfma_f32_16x16x32_bf16 v[120:123], v[144:147], v[140:143], v[120:123]\n"
"ds_read_b64 v[178:179], v227 offset:3272\n"
"ds_read_b128 v[180:183], v228 offset:3264\n"
"v_mfma_f32_16x16x32_bf16 v[124:127], v[144:147], v[148:151], v[124:127]\n"
"ds_read_b64 v[184:185], v227 offset:3776\n"
"ds_read_b64 v[186:187], v227 offset:3784\n"
"s_waitcnt lgkmcnt(8)\n"
"v_mfma_f32_16x16x32_bf16 v[112:115], v[152:155], v[132:135], 0\n"
"v_mfma_f32_16x16x32_bf16 v[116:119], v[152:155], v[140:143], v[116:119]\n"
"v_mfma_f32_16x16x32_bf16 v[120:123], v[152:155], v[148:151], v[120:123]\n"
"v_mfma_f32_16x16x32_bf16 v[124:127], v[152:155], v[156:159], v[124:127]\n"
"v_mfma_f32_16x16x32_bf16 v[108:111], v[160:163], v[132:135], 0\n"
"ds_read_b128 v[188:191], v228 offset:3776\n"
"v_mfma_f32_16x16x32_bf16 v[112:115], v[160:163], v[140:143], v[112:115]\n"
"ds_read_b64 v[128:129], v227 offset:128\n"
"ds_read_b64 v[130:131], v227 offset:136\n"
"v_mfma_f32_16x16x32_bf16 v[116:119], v[160:163], v[148:151], v[116:119]\n"
"ds_read_b128 v[136:139], v228 offset:128\n"
"ds_read_b64 v[144:145], v227 offset:640\n"
"v_mfma_f32_16x16x32_bf16 v[120:123], v[160:163], v[156:159], v[120:123]\n"
"ds_read_b64 v[146:147], v227 offset:648\n"
"ds_read_b128 v[152:155], v228 offset:640\n"
"v_mfma_f32_16x16x32_bf16 v[124:127], v[160:163], v[164:167], v[124:127]\n"
"ds_read_b64 v[160:161], v227 offset:1152\n"
"ds_read_b64 v[162:163], v227 offset:1160\n"
"s_waitcnt lgkmcnt(8)\n"
"v_mfma_f32_16x16x32_bf16 v[104:107], v[168:171], v[132:135], 0\n"
"v_mfma_f32_16x16x32_bf16 v[108:111], v[168:171], v[140:143], v[108:111]\n"
"v_mfma_f32_16x16x32_bf16 v[112:115], v[168:171], v[148:151], v[112:115]\n"
"v_mfma_f32_16x16x32_bf16 v[116:119], v[168:171], v[156:159], v[116:119]\n"
"v_mfma_f32_16x16x32_bf16 v[120:123], v[168:171], v[164:167], v[120:123]\n"
"v_mfma_f32_16x16x32_bf16 v[124:127], v[168:171], v[172:175], v[124:127]\n"
"ds_read_b128 v[168:171], v228 offset:1152\n"
"v_mfma_f32_16x16x32_bf16 v[100:103], v[176:179], v[132:135], 0\n"
"v_mfma_f32_16x16x32_bf16 v[104:107], v[176:179], v[140:143], v[104:107]\n"
"v_mfma_f32_16x16x32_bf16 v[108:111], v[176:179], v[148:151], v[108:111]\n"
"v_mfma_f32_16x16x32_bf16 v[112:115], v[176:179], v[156:159], v[112:115]\n"
"v_mfma_f32_16x16x32_bf16 v[116:119], v[176:179], v[164:167], v[116:119]\n"
"v_mfma_f32_16x16x32_bf16 v[120:123], v[176:179], v[172:175], v[120:123]\n"
"v_mfma_f32_16x16x32_bf16 v[124:127], v[176:179], v[180:183], v[124:127]\n"
"ds_read_b64 v[176:177], v227 offset:1664\n"
"ds_read_b64 v[178:179], v227 offset:1672\n"
"v_mfma_f32_16x16x32_bf16 v[96:99], v[184:187], v[132:135], 0\n"
"ds_read_b128 v[132:135], v228 offset:1664\n"
"v_mfma_f32_16x16x32_bf16 v[100:103], v[184:187], v[140:143], v[100:103]\n"
"ds_read_b64 v[140:141], v227 offset:2176\n"
"ds_read_b64 v[142:143], v227 offset:2184\n"
"v_mfma_f32_16x16x32_bf16 v[104:107], v[184:187], v[148:151], v[104:107]\n"
"ds_read_b128 v[148:151], v228 offset:2176\n"
"v_mfma_f32_16x16x32_bf16 v[108:111], v[184:187], v[156:159], v[108:111]\n"
"ds_read_b64 v[156:157], v227 offset:2688\n"
"ds_read_b64 v[158:159], v227 offset:2696\n"
"v_mfma_f32_16x16x32_bf16 v[112:115], v[184:187], v[164:167], v[112:115]\n"
"ds_read_b128 v[164:167], v228 offset:2688\n"
"v_mfma_f32_16x16x32_bf16 v[116:119], v[184:187], v[172:175], v[116:119]\n"
"ds_read_b64 v[172:173], v227 offset:3200\n"
"ds_read_b64 v[174:175], v227 offset:3208\n"
"v_mfma_f32_16x16x32_bf16 v[120:123], v[184:187], v[180:183], v[120:123]\n"
"v_mfma_f32_16x16x32_bf16 v[124:127], v[184:187], v[188:191], v[124:127]\n"
"s_waitcnt lgkmcnt(6)\n"
"v_mfma_f32_16x16x32_bf16 v[124:127], v[128:131], v[136:139], v[124:127]\n"
"v_mfma_f32_16x16x32_bf16 v[120:123], v[144:147], v[136:139], v[120:123]\n"
"v_mfma_f32_16x16x32_bf16 v[124:127], v[144:147], v[152:155], v[124:127]\n"
"ds_read_b128 v[180:183], v228 offset:3200\n"
"v_mfma_f32_16x16x32_bf16 v[116:119], v[160:163], v[136:139], v[116:119]\n"
"ds_read_b64 v[184:185], v227 offset:3712\n"
"ds_read_b64 v[186:187], v227 offset:3720\n"
"v_mfma_f32_16x16x32_bf16 v[120:123], v[160:163], v[152:155], v[120:123]\n"
"v_mfma_f32_16x16x32_bf16 v[124:127], v[160:163], v[168:171], v[124:127]\n"
"v_mfma_f32_16x16x32_bf16 v[112:115], v[176:179], v[136:139], v[112:115]\n"
"v_mfma_f32_16x16x32_bf16 v[116:119], v[176:179], v[152:155], v[116:119]\n"
"v_mfma_f32_16x16x32_bf16 v[120:123], v[176:179], v[168:171], v[120:123]\n"
"v_mfma_f32_16x16x32_bf16 v[124:127], v[176:179], v[132:135], v[124:127]\n"
"v_mfma_f32_16x16x32_bf16 v[108:111], v[140:143], v[136:139], v[108:111]\n"
"ds_read_b128 v[188:191], v228 offset:3712\n"
"v_mfma_f32_16x16x32_bf16 v[112:115], v[140:143], v[152:155], v[112:115]\n"
"ds_read_b64 v[128:129], v227 offset:64\n"
"ds_read_b64 v[130:131], v227 offset:72\n"
"v_mfma_f32_16x16x32_bf16 v[116:119], v[140:143], v[168:171], v[116:119]\n"
"ds_read_b128 v[144:147], v228 offset:64\n"
"ds_read_b64 v[160:161], v227 offset:576\n"
"v_mfma_f32_16x16x32_bf16 v[120:123], v[140:143], v[132:135], v[120:123]\n"
"ds_read_b64 v[162:163], v227 offset:584\n"
"ds_read_b128 v[176:179], v228 offset:576\n"
"s_waitcnt lgkmcnt(7)\n"
"v_mfma_f32_16x16x32_bf16 v[124:127], v[140:143], v[148:151], v[124:127]\n"
"ds_read_b64 v[140:141], v227 offset:1088\n"
"ds_read_b64 v[142:143], v227 offset:1096\n"
"v_mfma_f32_16x16x32_bf16 v[104:107], v[156:159], v[136:139], v[104:107]\n"
"v_mfma_f32_16x16x32_bf16 v[108:111], v[156:159], v[152:155], v[108:111]\n"
"v_mfma_f32_16x16x32_bf16 v[112:115], v[156:159], v[168:171], v[112:115]\n"
"v_mfma_f32_16x16x32_bf16 v[116:119], v[156:159], v[132:135], v[116:119]\n"
"v_mfma_f32_16x16x32_bf16 v[120:123], v[156:159], v[148:151], v[120:123]\n"
"v_mfma_f32_16x16x32_bf16 v[124:127], v[156:159], v[164:167], v[124:127]\n"
"ds_read_b128 v[156:159], v228 offset:1088\n"
"v_mfma_f32_16x16x32_bf16 v[100:103], v[172:175], v[136:139], v[100:103]\n"
"v_mfma_f32_16x16x32_bf16 v[104:107], v[172:175], v[152:155], v[104:107]\n"
"v_mfma_f32_16x16x32_bf16 v[108:111], v[172:175], v[168:171], v[108:111]\n"
"v_mfma_f32_16x16x32_bf16 v[112:115], v[172:175], v[132:135], v[112:115]\n"
"v_mfma_f32_16x16x32_bf16 v[116:119], v[172:175], v[148:151], v[116:119]\n"
"v_mfma_f32_16x16x32_bf16 v[120:123], v[172:175], v[164:167], v[120:123]\n"
"v_mfma_f32_16x16x32_bf16 v[124:127], v[172:175], v[180:183], v[124:127]\n"
"ds_read_b64 v[172:173], v227 offset:1600\n"
"ds_read_b64 v[174:175], v227 offset:1608\n"
"v_mfma_f32_16x16x32_bf16 v[96:99], v[184:187], v[136:139], v[96:99]\n"
"ds_read_b128 v[136:139], v228 offset:1600\n"
"v_mfma_f32_16x16x32_bf16 v[100:103], v[184:187], v[152:155], v[100:103]\n"
"ds_read_b64 v[152:153], v227 offset:2112\n"
"ds_read_b64 v[154:155], v227 offset:2120\n"
"v_mfma_f32_16x16x32_bf16 v[104:107], v[184:187], v[168:171], v[104:107]\n"
"ds_read_b128 v[168:171], v228 offset:2112\n"
"v_mfma_f32_16x16x32_bf16 v[108:111], v[184:187], v[132:135], v[108:111]\n"
"ds_read_b64 v[132:133], v227 offset:2624\n"
"ds_read_b64 v[134:135], v227 offset:2632\n"
"v_mfma_f32_16x16x32_bf16 v[112:115], v[184:187], v[148:151], v[112:115]\n"
"ds_read_b128 v[148:151], v228 offset:2624\n"
"v_mfma_f32_16x16x32_bf16 v[116:119], v[184:187], v[164:167], v[116:119]\n"
"ds_read_b64 v[164:165], v227 offset:3136\n"
"ds_read_b64 v[166:167], v227 offset:3144\n"
"v_mfma_f32_16x16x32_bf16 v[120:123], v[184:187], v[180:183], v[120:123]\n"
"s_waitcnt lgkmcnt(6)\n"
"v_mfma_f32_16x16x32_bf16 v[124:127], v[184:187], v[188:191], v[124:127]\n"
"v_mfma_f32_16x16x32_bf16 v[124:127], v[128:131], v[144:147], v[124:127]\n"
"v_mfma_f32_16x16x32_bf16 v[120:123], v[160:163], v[144:147], v[120:123]\n"
"v_mfma_f32_16x16x32_bf16 v[124:127], v[160:163], v[176:179], v[124:127]\n"
"ds_read_b128 v[180:183], v228 offset:3136\n"
"v_mfma_f32_16x16x32_bf16 v[116:119], v[140:143], v[144:147], v[116:119]\n"
"ds_read_b64 v[184:185], v227 offset:3648\n"
"ds_read_b64 v[186:187], v227 offset:3656\n"
"v_mfma_f32_16x16x32_bf16 v[120:123], v[140:143], v[176:179], v[120:123]\n"
"v_mfma_f32_16x16x32_bf16 v[124:127], v[140:143], v[156:159], v[124:127]\n"
"v_mfma_f32_16x16x32_bf16 v[112:115], v[172:175], v[144:147], v[112:115]\n"
"v_mfma_f32_16x16x32_bf16 v[116:119], v[172:175], v[176:179], v[116:119]\n"
"v_mfma_f32_16x16x32_bf16 v[120:123], v[172:175], v[156:159], v[120:123]\n"
"v_mfma_f32_16x16x32_bf16 v[124:127], v[172:175], v[136:139], v[124:127]\n"
"v_mfma_f32_16x16x32_bf16 v[108:111], v[152:155], v[144:147], v[108:111]\n"
"ds_read_b128 v[188:191], v228 offset:3648\n"
"v_mfma_f32_16x16x32_bf16 v[112:115], v[152:155], v[176:179], v[112:115]\n"
"ds_read_b64 v[128:129], v227\n"
"ds_read_b64 v[130:131], v227 offset:8\n"
"v_mfma_f32_16x16x32_bf16 v[116:119], v[152:155], v[156:159], v[116:119]\n"
"ds_read_b128 v[160:163], v228\n"
"ds_read_b64 v[140:141], v227 offset:512\n"
"v_mfma_f32_16x16x32_bf16 v[120:123], v[152:155], v[136:139], v[120:123]\n"
"ds_read_b64 v[142:143], v227 offset:520\n"
"ds_read_b128 v[172:175], v228 offset:512\n"
"s_waitcnt lgkmcnt(7)\n"
"v_mfma_f32_16x16x32_bf16 v[124:127], v[152:155], v[168:171], v[124:127]\n"
"ds_read_b64 v[152:153], v227 offset:1024\n"
"ds_read_b64 v[154:155], v227 offset:1032\n"
"v_mfma_f32_16x16x32_bf16 v[104:107], v[132:135], v[144:147], v[104:107]\n"
"v_mfma_f32_16x16x32_bf16 v[108:111], v[132:135], v[176:179], v[108:111]\n"
"v_mfma_f32_16x16x32_bf16 v[112:115], v[132:135], v[156:159], v[112:115]\n"
"v_mfma_f32_16x16x32_bf16 v[116:119], v[132:135], v[136:139], v[116:119]\n"
"v_mfma_f32_16x16x32_bf16 v[120:123], v[132:135], v[168:171], v[120:123]\n"
"v_mfma_f32_16x16x32_bf16 v[124:127], v[132:135], v[148:151], v[124:127]\n"
"ds_read_b128 v[132:135], v228 offset:1024\n"
"v_mfma_f32_16x16x32_bf16 v[100:103], v[164:167], v[144:147], v[100:103]\n"
"v_mfma_f32_16x16x32_bf16 v[104:107], v[164:167], v[176:179], v[104:107]\n"
"v_mfma_f32_16x16x32_bf16 v[108:111], v[164:167], v[156:159], v[108:111]\n"
"v_mfma_f32_16x16x32_bf16 v[112:115], v[164:167], v[136:139], v[112:115]\n"
"v_mfma_f32_16x16x32_bf16 v[116:119], v[164:167], v[168:171], v[116:119]\n"
"v_mfma_f32_16x16x32_bf16 v[120:123], v[164:167], v[148:151], v[120:123]\n"
"v_mfma_f32_16x16x32_bf16 v[124:127], v[164:167], v[180:183], v[124:127]\n"
"ds_read_b64 v[164:165], v227 offset:1536\n"
"ds_read_b64 v[166:167], v227 offset:1544\n"
"v_mfma_f32_16x16x32_bf16 v[96:99], v[184:187], v[144:147], v[96:99]\n"
"ds_read_b128 v[144:147], v228 offset:1536\n"
"v_mfma_f32_16x16x32_bf16 v[100:103], v[184:187], v[176:179], v[100:103]\n"
"ds_read_b64 v[176:177], v227 offset:2048\n"
"ds_read_b64 v[178:179], v227 offset:2056\n"
"v_mfma_f32_16x16x32_bf16 v[104:107], v[184:187], v[156:159], v[104:107]\n"
"ds_read_b128 v[156:159], v228 offset:2048\n"
"v_mfma_f32_16x16x32_bf16 v[108:111], v[184:187], v[136:139], v[108:111]\n"
"ds_read_b64 v[136:137], v227 offset:2560\n"
"ds_read_b64 v[138:139], v227 offset:2568\n"
"v_mfma_f32_16x16x32_bf16 v[112:115], v[184:187], v[168:171], v[112:115]\n"
"ds_read_b128 v[168:171], v228 offset:2560\n"
"v_mfma_f32_16x16x32_bf16 v[116:119], v[184:187], v[148:151], v[116:119]\n"
"ds_read_b64 v[148:149], v227 offset:3072\n"
"ds_read_b64 v[150:151], v227 offset:3080\n"
"v_mfma_f32_16x16x32_bf16 v[120:123], v[184:187], v[180:183], v[120:123]\n"
"s_waitcnt lgkmcnt(6)\n"
"v_mfma_f32_16x16x32_bf16 v[124:127], v[184:187], v[188:191], v[124:127]\n"
"v_mfma_f32_16x16x32_bf16 v[124:127], v[128:131], v[160:163], v[124:127]\n"
"v_mfma_f32_16x16x32_bf16 v[120:123], v[140:143], v[160:163], v[120:123]\n"
"v_mfma_f32_16x16x32_bf16 v[124:127], v[140:143], v[172:175], v[124:127]\n"
"ds_read_b128 v[180:183], v228 offset:3072\n"
"v_mfma_f32_16x16x32_bf16 v[116:119], v[152:155], v[160:163], v[116:119]\n"
"ds_read_b64 v[184:185], v227 offset:3584\n"
"ds_read_b64 v[186:187], v227 offset:3592\n"
"v_mfma_f32_16x16x32_bf16 v[120:123], v[152:155], v[172:175], v[120:123]\n"
"v_mfma_f32_16x16x32_bf16 v[124:127], v[152:155], v[132:135], v[124:127]\n"
"v_mfma_f32_16x16x32_bf16 v[112:115], v[164:167], v[160:163], v[112:115]\n"
"v_mfma_f32_16x16x32_bf16 v[116:119], v[164:167], v[172:175], v[116:119]\n"
"v_mfma_f32_16x16x32_bf16 v[120:123], v[164:167], v[132:135], v[120:123]\n"
"v_mfma_f32_16x16x32_bf16 v[124:127], v[164:167], v[144:147], v[124:127]\n"
"v_mfma_f32_16x16x32_bf16 v[108:111], v[176:179], v[160:163], v[108:111]\n"
"ds_read_b128 v[188:191], v228 offset:3584\n"
"v_mfma_f32_16x16x32_bf16 v[112:115], v[176:179], v[172:175], v[112:115]\n"
"v_mfma_f32_16x16x32_bf16 v[116:119], v[176:179], v[132:135], v[116:119]\n"
"v_mfma_f32_16x16x32_bf16 v[120:123], v[176:179], v[144:147], v[120:123]\n"
"s_waitcnt lgkmcnt(1)\n"
"v_mfma_f32_16x16x32_bf16 v[124:127], v[176:179], v[156:159], v[124:127]\n"
"v_mfma_f32_16x16x32_bf16 v[104:107], v[136:139], v[160:163], v[104:107]\n"
"v_mfma_f32_16x16x32_bf16 v[108:111], v[136:139], v[172:175], v[108:111]\n"
"v_mfma_f32_16x16x32_bf16 v[112:115], v[136:139], v[132:135], v[112:115]\n"
"v_mfma_f32_16x16x32_bf16 v[116:119], v[136:139], v[144:147], v[116:119]\n"
"v_mfma_f32_16x16x32_bf16 v[120:123], v[136:139], v[156:159], v[120:123]\n"
"v_mfma_f32_16x16x32_bf16 v[124:127], v[136:139], v[168:171], v[124:127]\n"
"v_mfma_f32_16x16x32_bf16 v[100:103], v[148:151], v[160:163], v[100:103]\n"
"v_mfma_f32_16x16x32_bf16 v[104:107], v[148:151], v[172:175], v[104:107]\n"
"v_mfma_f32_16x16x32_bf16 v[108:111], v[148:151], v[132:135], v[108:111]\n"
"v_mfma_f32_16x16x32_bf16 v[112:115], v[148:151], v[144:147], v[112:115]\n"
"v_mfma_f32_16x16x32_bf16 v[116:119], v[148:151], v[156:159], v[116:119]\n"
"v_mfma_f32_16x16x32_bf16 v[120:123], v[148:151], v[168:171], v[120:123]\n"
"v_mfma_f32_16x16x32_bf16 v[124:127], v[148:151], v[180:183], v[124:127]\n"
"v_mfma_f32_16x16x32_bf16 v[96:99], v[184:187], v[160:163], v[96:99]\n"
"v_mfma_f32_16x16x32_bf16 v[100:103], v[184:187], v[172:175], v[100:103]\n"
"v_mfma_f32_16x16x32_bf16 v[104:107], v[184:187], v[132:135], v[104:107]\n"
"v_mfma_f32_16x16x32_bf16 v[108:111], v[184:187], v[144:147], v[108:111]\n"
"v_mfma_f32_16x16x32_bf16 v[112:115], v[184:187], v[156:159], v[112:115]\n"
"v_mfma_f32_16x16x32_bf16 v[116:119], v[184:187], v[168:171], v[116:119]\n"
"v_mfma_f32_16x16x32_bf16 v[120:123], v[184:187], v[180:183], v[120:123]\n"
"s_waitcnt lgkmcnt(0)\n"
"v_mfma_f32_16x16x32_bf16 v[124:127], v[184:187], v[188:191], v[124:127]\n"
"s_setprio 3\n"
"s_mov_b64 exec, 1\n"
"ds_add_u32 v248, v249\n"
"s_mov_b64 exec, -1\n"
"ds_read_b32 v250, v248\n"
"v_mov_b32 v227, 0x27e70\n"
"v_mov_b32 v228, 0x27e40\n"
"s_mov_b64 exec, 1\n"
"ds_add_u32 v227, v249\n"
"s_mov_b64 exec, -1\n"
"s_waitcnt lgkmcnt(1)\n"
"v_cmp_gt_u32 vcc, 8, v250\n"
"s_cbranch_vccnz .Lslow_c20\n"
".Lret_c20:\n"
"ds_write_b128 v224, v[96:99]\n"
"ds_write_b128 v224, v[100:103] offset:1024\n"
"ds_write_b128 v224, v[104:107] offset:2048\n"
"ds_write_b128 v224, v[108:111] offset:3072\n"
"ds_write_b128 v224, v[112:115] offset:4096\n"
"ds_write_b128 v224, v[116:119] offset:5120\n"
"ds_write_b128 v224, v[120:123] offset:6144\n"
"ds_write_b128 v224, v[124:127] offset:7168\n"
"s_waitcnt lgkmcnt(0)\n"
"s_mov_b64 exec, 1\n"
"ds_add_u32 v248, v249\n"
"s_mov_b64 exec, -1\n"
"ds_read_b32 v250, v248\n"
"s_lshr_b32 s76, s64, 4\n"
"s_lshl_b32 s75, s65, 12\n"
"s_lshl_b32 s77, s67, 11\n"
"s_add_u32 s75, s75, s77\n"
"s_lshl_b32 s77, s76, 4\n"
"s_add_u32 s75, s75, s77\n"
"s_add_u32 s75, s75, 0xb43000\n"
"v_bfe_u32 v251, v232, 1, 4\n"
"v_and_b32 v252, 1, v232\n"
"v_lshl_add_u32 v251, v251, 4, v252\n"
"v_lshl_add_u32 v154, v251, 3, s75\n"
"s_waitcnt lgkmcnt(0)\n"
"v_cmp_gt_u32 vcc, 10, v250\n"
"s_cbranch_vccnz .Lslow_c21\n"
".Lret_c21:\n"
"ds_read_b128 v[160:163], v225\n"
"ds_read_b128 v[164:167], v226\n"
"ds_read_b128 v[168:171], v229\n"
"ds_read_b128 v[172:175], v231\n"
"ds_read_b128 v[176:179], v225 offset:2048\n"
"ds_read_b128 v[180:183], v226 offset:2048\n"
"ds_read_b128 v[184:187], v229 offset:2048\n"
"ds_read_b128 v[188:191], v231 offset:2048\n"
"s_waitcnt lgkmcnt(5)\n"
"v_add_f32 v160, v160, v168\n"
"v_add_f32 v161, v161, v169\n"
"v_add_f32 v162, v162, v170\n"
"v_add_f32 v163, v163, v171\n"
"s_waitcnt lgkmcnt(4)\n"
"v_add_f32 v164, v164, v172\n"
"v_add_f32 v165, v165, v173\n"
"v_add_f32 v166, v166, v174\n"
"v_add_f32 v167, v167, v175\n"
"v_cvt_pk_bf16_f32 v160, v160, v161\n"
"v_cvt_pk_bf16_f32 v161, v162, v163\n"
"v_cvt_pk_bf16_f32 v162, v164, v165\n"
"v_cvt_pk_bf16_f32 v163, v166, v167\n"
"buffer_store_dwordx4 v[160:163], v230, s[52:55], s92 offen sc1\n"
"s_waitcnt lgkmcnt(0)\n"
"v_add_f32 v176, v176, v184\n"
"v_add_f32 v177, v177, v185\n"
"v_add_f32 v178, v178, v186\n"
"v_add_f32 v179, v179, v187\n"
"v_add_f32 v180, v180, v188\n"
"v_add_f32 v181, v181, v189\n"
"v_add_f32 v182, v182, v190\n"
"v_add_f32 v183, v183, v191\n"
"v_cvt_pk_bf16_f32 v176, v176, v177\n"
"v_cvt_pk_bf16_f32 v177, v178, v179\n"
"v_cvt_pk_bf16_f32 v178, v180, v181\n"
"v_cvt_pk_bf16_f32 v179, v182, v183\n"
"buffer_store_dwordx4 v[176:179], v230, s[52:55], s92 offen offset:1024 sc1\n"
"ds_read_b32 v250, v227\n"
"s_mul_i32 s75, s65, 0x42000\n"
"s_mul_i32 s77, s67, 0x21000\n"
"s_add_u32 s75, s75, s77\n"
"s_lshl_b32 s77, s64, 7\n"
"s_add_u32 s75, s75, s77\n"
"s_lshl_b32 s77, s66, 6\n"
"s_add_u32 s75, s75, s77\n"
"s_add_u32 s75, s75, 0x200000\n"
"v_lshrrev_b32 v251, 2, v232\n"
"v_mul_u32_u24 v251, 0x1080, v251\n"
"v_and_b32 v252, 3, v232\n"
"v_lshl_add_u32 v251, v252, 4, v251\n"
"v_add_u32 v128, s75, v251\n"
"s_mov_b32 s92, 0x10800\n"
"s_lshl_b32 s75, s97, 11\n"
"s_add_u32 s75, s75, 0x20800\n"
"v_lshl_add_u32 v129, v232, 4, s75\n"
"v_lshrrev_b32 v251, 5, v232\n"
"v_bfe_u32 v253, v232, 2, 2\n"
"v_lshl_add_u32 v253, v251, 2, v253\n"
"v_lshlrev_b32 v253, 6, v253\n"
"v_lshl_add_u32 v253, v252, 3, v253\n"
"v_bfe_u32 v252, v232, 4, 1\n"
"v_lshl_add_u32 v253, v252, 5, v253\n"
"v_add_u32 v130, s75, v253\n"
"s_lshl_b32 s75, s66, 12\n"
"s_add_u32 s75, s75, 0x22800\n"
"s_lshl_b32 s76, s67, 3\n"
"v_or_b32 v251, s76, v251\n"
"v_and_b32 v252, 15, v232\n"
"v_xor_b32 v251, v251, v252\n"
"v_lshlrev_b32 v251, 3, v251\n"
"v_and_b32 v252, 31, v232\n"
"v_lshl_add_u32 v252, v252, 7, v251\n"
"v_add_u32 v131, s75, v252\n"
"v_xor_b32 v149, 16, v131\n"
"v_xor_b32 v150, 32, v131\n"
"v_xor_b32 v151, 48, v131\n"
"s_waitcnt vmcnt(0)\n"
"s_mov_b64 exec, 1\n"
"buffer_store_dwordx2 v[238:239], v233, s[52:55], s93 offen sc1\n"
"s_mov_b64 exec, -1\n"
"s_waitcnt lgkmcnt(0)\n"
"v_cmp_gt_u32 vcc, 4, v250\n"
"s_cbranch_vccnz .Lslow_c22\n"
".Lret_c22:\n"
"s_lshl_b32 s75, s64, 8\n"
"s_add_u32 s75, s75, 0x6400\n"
"v_lshl_add_u32 v40, v232, 2, s75\n"
"buffer_load_dword v41, v40, s[52:55], 0 offen sc1\n"
"v_lshlrev_b32 v40, 2, v232\n"
"v_add_u32 v40, 0x24800, v40\n"
"s_lshl_b32 s76, s97, 2\n"
"s_add_u32 s76, s76, 16\n"
"v_lshrrev_b32 v251, 4, v232\n"
"v_add_u32 v251, s76, v251\n"
"v_and_b32 v252, 15, v232\n"
"s_mul_i32 s75, s64, 0xe000\n"
"s_lshl_b32 s77, s65, 7\n"
"s_add_u32 s75, s75, s77\n"
"v_mul_u32_u24 v253, 0x380, v251\n"
"v_lshl_add_u32 v253, v252, 3, v253\n"
"v_add_u32 v0, s75, v253\n"
"buffer_load_dwordx2 v[4:5], v0, s[40:43], 0 offen\n"
"v_lshlrev_b32 v253, 2, v251\n"
"v_add_u32 v2, 0x24800, v253\n"
"v_and_b32 v253, 15, v251\n"
"v_xor_b32 v253, v253, v252\n"
"v_lshlrev_b32 v253, 3, v253\n"
"v_lshl_add_u32 v253, v251, 7, v253\n"
"v_add_u32 v3, 0x22800, v253\n"
"v_lshrrev_b32 v253, 3, v252\n"
"v_lshlrev_b32 v253, 2, v253\n"
"v_and_b32 v254, 1, v252\n"
"v_lshl_add_u32 v253, v254, 1, v253\n"
"v_bfe_u32 v254, v252, 2, 1\n"
"v_or_b32 v253, v253, v254\n"
"v_and_b32 v254, 7, v251\n"
"v_xor_b32 v253, v253, v254\n"
"v_lshlrev_b32 v253, 4, v253\n"
"v_bfe_u32 v254, v252, 1, 1\n"
"v_lshl_add_u32 v253, v254, 3, v253\n"
"v_lshl_add_u32 v253, v251, 7, v253\n"
"v_add_u32 v1, 0x8000, v253\n"
"s_lshl_b32 s76, s97, 2\n"
"s_add_u32 s76, s76, 16\n"
"v_lshrrev_b32 v251, 4, v232\n"
"v_add_u32 v251, s76, v251\n"
"v_and_b32 v252, 15, v232\n"
"s_mul_i32 s75, s64, 0xe000\n"
"s_lshl_b32 s77, s65, 7\n"
"s_add_u32 s75, s75, s77\n"
"s_add_u32 s75, s75, 0x7000\n"
"v_mul_u32_u24 v253, 0x380, v251\n"
"v_lshl_add_u32 v253, v252, 3, v253\n"
"v_add_u32 v8, s75, v253\n"
"buffer_load_dwordx2 v[12:13], v8, s[40:43], 0 offen\n"
"v_lshlrev_b32 v253, 2, v251\n"
"v_add_u32 v10, 0x24880, v253\n"
"v_and_b32 v253, 15, v251\n"
"v_xor_b32 v253, v253, v252\n"
"v_lshlrev_b32 v253, 3, v253\n"
"v_lshl_add_u32 v253, v251, 7, v253\n"
"v_add_u32 v11, 0x23800, v253\n"
"v_lshrrev_b32 v253, 3, v252\n"
"v_lshlrev_b32 v253, 2, v253\n"
"v_and_b32 v254, 1, v252\n"
"v_lshl_add_u32 v253, v254, 1, v253\n"
"v_bfe_u32 v254, v252, 2, 1\n"
"v_or_b32 v253, v253, v254\n"
"v_and_b32 v254, 7, v251\n"
"v_xor_b32 v253, v253, v254\n"
"v_lshlrev_b32 v253, 4, v253\n"
"v_bfe_u32 v254, v252, 1, 1\n"
"v_lshl_add_u32 v253, v254, 3, v253\n"
"v_lshl_add_u32 v253, v251, 7, v253\n"
"v_add_u32 v9, 0xc000, v253\n"
"s_mov_b32 s82, 0x3f80\n"
"s_mov_b32 s83, 0x3f800000\n"
"s_waitcnt vmcnt(2)\n"
"ds_write_b32 v40, v41\n"
"s_setprio 0\n"
"v_mov_b32 v255, 0x27e78\n"
"ds_read_b32 v250, v255\n"
"s_waitcnt lgkmcnt(0)\n"
"v_cmp_gt_u32 vcc, 1, v250\n"
"s_cbranch_vccnz .Lslow_c23\n"
".Lret_c23:\n"
"s_setprio 3\n"
"buffer_load_dwordx2 v[152:153], v154, s[52:55], 0 offen sc1\n"
"s_setprio 0\n"
"s_waitcnt vmcnt(0)\n"
"v_cmp_ne_u64 vcc, s[60:61], v[152:153]\n"
"s_cbranch_vccnz .Lslow_g24\n"
".Lret_g24:\n"
"s_setprio 3\n"
"buffer_load_dwordx4 v[132:135], v128, s[52:55], 0 offen sc1\n"
"buffer_load_dwordx4 v[136:139], v128, s[52:55], s92 offen sc1\n"
"s_waitcnt vmcnt(1)\n"
"ds_write_b128 v129, v[132:135]\n"
"s_waitcnt vmcnt(0)\n"
"ds_write_b128 v129, v[136:139] offset:1024\n"
"ds_read_b64_tr_b16 v[140:141], v130\n"
"ds_read_b64_tr_b16 v[142:143], v130 offset:512\n"
"ds_read_b64_tr_b16 v[144:145], v130 offset:1024\n"
"ds_read_b64_tr_b16 v[146:147], v130 offset:1536\n"
"s_waitcnt lgkmcnt(3)\n"
"ds_write_b64 v131, v[140:141]\n"
"s_waitcnt lgkmcnt(3)\n"
"ds_write_b64 v149, v[142:143]\n"
"s_waitcnt lgkmcnt(3)\n"
"ds_write_b64 v150, v[144:145]\n"
"s_waitcnt lgkmcnt(3)\n"
"ds_write_b64 v151, v[146:147]\n"
"v_lshlrev_b32 v16, 16, v4\n"
"v_and_b32 v17, s39, v4\n"
"v_lshlrev_b32 v18, 16, v5\n"
"v_and_b32 v19, s39, v5\n"
"v_lshlrev_b32 v24, 16, v12\n"
"v_and_b32 v25, s39, v12\n"
"v_lshlrev_b32 v26, 16, v13\n"
"v_and_b32 v27, s39, v13\n"
"s_waitcnt lgkmcnt(0)\n"
"s_barrier\n"
"ds_read_b64 v[32:33], v1\n"
"ds_read_b64 v[34:35], v1 offset:4096\n"
"ds_read_b64 v[36:37], v1 offset:8192\n"
"ds_read_b64 v[38:39], v1 offset:12288\n"
"ds_read_b64 v[40:41], v3\n"
"ds_read_b32 v6, v2\n"
"ds_read_b64 v[48:49], v9\n"
"ds_read_b64 v[50:51], v9 offset:4096\n"
"ds_read_b64 v[52:53], v9 offset:8192\n"
"ds_read_b64 v[54:55], v9 offset:12288\n"
"ds_read_b64 v[56:57], v11\n"
"ds_read_b32 v14, v10\n"
"s_waitcnt lgkmcnt(11)\n"
"v_lshlrev_b32 v44, 16, v32\n"
"v_and_b32 v45, s39, v32\n"
"v_lshlrev_b32 v46, 16, v33\n"
"v_and_b32 v47, s39, v33\n"
"s_waitcnt lgkmcnt(7)\n"
"v_dot2_f32_bf16 v44, v34, s82, v44\n"
"v_dot2_f32_bf16 v45, v34, s83, v45\n"
"v_dot2_f32_bf16 v46, v35, s82, v46\n"
"v_dot2_f32_bf16 v47, v35, s83, v47\n"
"v_dot2_f32_bf16 v44, v36, s82, v44\n"
"v_dot2_f32_bf16 v45, v36, s83, v45\n"
"v_dot2_f32_bf16 v46, v37, s82, v46\n"
"v_dot2_f32_bf16 v47, v37, s83, v47\n"
"v_dot2_f32_bf16 v44, v38, s82, v44\n"
"v_dot2_f32_bf16 v45, v38, s83, v45\n"
"v_dot2_f32_bf16 v46, v39, s82, v46\n"
"v_dot2_f32_bf16 v47, v39, s83, v47\n"
"v_dot2_f32_bf16 v16, v40, s82, v16\n"
"v_dot2_f32_bf16 v17, v40, s83, v17\n"
"v_dot2_f32_bf16 v18, v41, s82, v18\n"
"v_dot2_f32_bf16 v19, v41, s83, v19\n"
"s_waitcnt lgkmcnt(0)\n"
"v_fma_f32 v44, v44, v6, v16\n"
"v_fma_f32 v45, v45, v6, v17\n"
"v_fma_f32 v46, v46, v6, v18\n"
"v_fma_f32 v47, v47, v6, v19\n"
"v_cvt_pk_bf16_f32 v44, v44, v45\n"
"v_cvt_pk_bf16_f32 v45, v46, v47\n"
"buffer_store_dwordx2 v[44:45], v0, s[56:59], 0 offen sc1\n"
"v_lshlrev_b32 v60, 16, v48\n"
"v_and_b32 v61, s39, v48\n"
"v_lshlrev_b32 v62, 16, v49\n"
"v_and_b32 v63, s39, v49\n"
"v_dot2_f32_bf16 v60, v50, s82, v60\n"
"v_dot2_f32_bf16 v61, v50, s83, v61\n"
"v_dot2_f32_bf16 v62, v51, s82, v62\n"
"v_dot2_f32_bf16 v63, v51, s83, v63\n"
"v_dot2_f32_bf16 v60, v52, s82, v60\n"
"v_dot2_f32_bf16 v61, v52, s83, v61\n"
"v_dot2_f32_bf16 v62, v53, s82, v62\n"
"v_dot2_f32_bf16 v63, v53, s83, v63\n"
"v_dot2_f32_bf16 v60, v54, s82, v60\n"
"v_dot2_f32_bf16 v61, v54, s83, v61\n"
"v_dot2_f32_bf16 v62, v55, s82, v62\n"
"v_dot2_f32_bf16 v63, v55, s83, v63\n"
"v_dot2_f32_bf16 v24, v56, s82, v24\n"
"v_dot2_f32_bf16 v25, v56, s83, v25\n"
"v_dot2_f32_bf16 v26, v57, s82, v26\n"
"v_dot2_f32_bf16 v27, v57, s83, v27\n"
"v_fma_f32 v60, v60, v14, v24\n"
"v_fma_f32 v61, v61, v14, v25\n"
"v_fma_f32 v62, v62, v14, v26\n"
"v_fma_f32 v63, v63, v14, v27\n"
"v_cvt_pk_bf16_f32 v60, v60, v61\n"
"v_cvt_pk_bf16_f32 v61, v62, v63\n"
"buffer_store_dwordx2 v[60:61], v8, s[56:59], 0 offen sc1\n"
"s_endpgm\n"
".Lslow_c1:\n"
"s_mov_b32 s90, 0x100000\n"
".Lspin_c1:\n"
"ds_read_b32 v250, v248\n"
"s_waitcnt lgkmcnt(0)\n"
"v_cmp_gt_u32 vcc, 4, v250\n"
"s_sub_u32 s90, s90, 1\n"
"s_cmp_eq_u32 s90, 0\n"
"s_cbranch_scc1 .Lret_c1\n"
"s_cbranch_vccnz .Lspin_c1\n"
"s_nop 4\n"
"s_branch .Lret_c1\n"
".Lslow_c2:\n"
"s_mov_b32 s90, 0x100000\n"
".Lspin_c2:\n"
"ds_read_b32 v250, v248\n"
"s_waitcnt lgkmcnt(0)\n"
"v_cmp_gt_u32 vcc, 8, v250\n"
"s_sub_u32 s90, s90, 1\n"
"s_cmp_eq_u32 s90, 0\n"
"s_cbranch_scc1 .Lret_c2\n"
"s_cbranch_vccnz .Lspin_c2\n"
"s_nop 4\n"
"s_branch .Lret_c2\n"
".Lslow_c3:\n"
"s_mov_b32 s90, 0x100000\n"
".Lspin_c3:\n"
"ds_read_b32 v250, v248\n"
"s_waitcnt lgkmcnt(0)\n"
"v_cmp_gt_u32 vcc, 12, v250\n"
"s_sub_u32 s90, s90, 1\n"
"s_cmp_eq_u32 s90, 0\n"
"s_cbranch_scc1 .Lret_c3\n"
"s_cbranch_vccnz .Lspin_c3\n"
"s_nop 4\n"
"s_branch .Lret_c3\n"
".Lslow_c4:\n"
"s_mov_b32 s90, 0x100000\n"
".Lspin_c4:\n"
"ds_read_b32 v250, v248\n"
"s_waitcnt lgkmcnt(0)\n"
"v_cmp_gt_u32 vcc, 16, v250\n"
"s_sub_u32 s90, s90, 1\n"
"s_cmp_eq_u32 s90, 0\n"
"s_cbranch_scc1 .Lret_c4\n"
"s_cbranch_vccnz .Lspin_c4\n"
"s_nop 4\n"
"s_branch .Lret_c4\n"
".Lslow_c5:\n"
"s_mov_b32 s90, 0x100000\n"
".Lspin_c5:\n"
"ds_read_b32 v250, v248\n"
"s_waitcnt lgkmcnt(0)\n"
"v_cmp_gt_u32 vcc, 20, v250\n"
"s_sub_u32 s90, s90, 1\n"
"s_cmp_eq_u32 s90, 0\n"
"s_cbranch_scc1 .Lret_c5\n"
"s_cbranch_vccnz .Lspin_c5\n"
"s_nop 4\n"
"s_branch .Lret_c5\n"
".Lslow_c6:\n"
"s_mov_b32 s90, 0x100000\n"
".Lspin_c6:\n"
"ds_read_b32 v250, v248\n"
"s_waitcnt lgkmcnt(0)\n"
"v_cmp_gt_u32 vcc, 24, v250\n"
"s_sub_u32 s90, s90, 1\n"
"s_cmp_eq_u32 s90, 0\n"
"s_cbranch_scc1 .Lret_c6\n"
"s_cbranch_vccnz .Lspin_c6\n"
"s_nop 4\n"
"s_branch .Lret_c6\n"
".Lslow_c7:\n"
"s_mov_b32 s90, 0x100000\n"
".Lspin_c7:\n"
"ds_read_b32 v250, v248\n"
"s_waitcnt lgkmcnt(0)\n"
"v_cmp_gt_u32 vcc, 28, v250\n"
"s_sub_u32 s90, s90, 1\n"
"s_cmp_eq_u32 s90, 0\n"
"s_cbranch_scc1 .Lret_c7\n"
"s_cbranch_vccnz .Lspin_c7\n"
"s_nop 4\n"
"s_branch .Lret_c7\n"
".Lslow_c8:\n"
"s_mov_b32 s90, 0x100000\n"
".Lspin_c8:\n"
"ds_read_b32 v250, v248\n"
"s_waitcnt lgkmcnt(0)\n"
"v_cmp_gt_u32 vcc, 32, v250\n"
"s_sub_u32 s90, s90, 1\n"
"s_cmp_eq_u32 s90, 0\n"
"s_cbranch_scc1 .Lret_c8\n"
"s_cbranch_vccnz .Lspin_c8\n"
"s_nop 4\n"
"s_branch .Lret_c8\n"
".Lslow_c9:\n"
"s_mov_b32 s90, 0x100000\n"
".Lspin_c9:\n"
"ds_read_b32 v250, v248\n"
"s_waitcnt lgkmcnt(0)\n"
"v_cmp_gt_u32 vcc, 36, v250\n"
"s_sub_u32 s90, s90, 1\n"
"s_cmp_eq_u32 s90, 0\n"
"s_cbranch_scc1 .Lret_c9\n"
"s_cbranch_vccnz .Lspin_c9\n"
"s_nop 4\n"
"s_branch .Lret_c9\n"
".Lslow_c10:\n"
"s_mov_b32 s90, 0x100000\n"
".Lspin_c10:\n"
"ds_read_b32 v250, v248\n"
"s_waitcnt lgkmcnt(0)\n"
"v_cmp_gt_u32 vcc, 40, v250\n"
"s_sub_u32 s90, s90, 1\n"
"s_cmp_eq_u32 s90, 0\n"
"s_cbranch_scc1 .Lret_c10\n"
"s_cbranch_vccnz .Lspin_c10\n"
"s_nop 4\n"
"s_branch .Lret_c10\n"
".Lslow_c11:\n"
"s_mov_b32 s90, 0x100000\n"
".Lspin_c11:\n"
"ds_read_b32 v250, v248\n"
"s_waitcnt lgkmcnt(0)\n"
"v_cmp_gt_u32 vcc, 44, v250\n"
"s_sub_u32 s90, s90, 1\n"
"s_cmp_eq_u32 s90, 0\n"
"s_cbranch_scc1 .Lret_c11\n"
"s_cbranch_vccnz .Lspin_c11\n"
"s_nop 4\n"
"s_branch .Lret_c11\n"
".Lslow_c12:\n"
"s_mov_b32 s90, 0x100000\n"
".Lspin_c12:\n"
"ds_read_b32 v250, v248\n"
"s_waitcnt lgkmcnt(0)\n"
"v_cmp_gt_u32 vcc, 48, v250\n"
"s_sub_u32 s90, s90, 1\n"
"s_cmp_eq_u32 s90, 0\n"
"s_cbranch_scc1 .Lret_c12\n"
"s_cbranch_vccnz .Lspin_c12\n"
"s_nop 4\n"
"s_branch .Lret_c12\n"
".Lslow_cs:\n"
"s_mov_b32 s90, 0x100000\n"
".Lspin_cs:\n"
"s_sleep 10\n"
"buffer_load_dwordx2 v[216:217], v236, s[52:55], 0 offen sc1\n"
"buffer_load_dwordx2 v[218:219], v236, s[52:55], 0 offen offset:256 sc1\n"
"buffer_load_dwordx2 v[220:221], v236, s[52:55], 0 offen offset:512 sc1\n"
"buffer_load_dwordx2 v[222:223], v236, s[52:55], 0 offen offset:768 sc1\n"
"buffer_load_dwordx2 v[208:209], v235, s[52:55], 0 offen sc1\n"
"buffer_load_dwordx2 v[212:213], v235, s[52:55], 0 offen offset:256 sc1\n"
"s_waitcnt vmcnt(0) lgkmcnt(0)\n"
"v_xor_b32 v251, s60, v217\n"
"v_xor_b32 v252, s60, v219\n"
"v_xor_b32 v253, s60, v221\n"
"v_or3_b32 v251, v251, v252, v253\n"
"v_xor_b32 v252, s60, v223\n"
"v_xor_b32 v253, s60, v209\n"
"v_or3_b32 v251, v251, v252, v253\n"
"v_xor_b32 v252, s60, v213\n"
"v_or_b32 v251, v251, v252\n"
"v_cmp_ne_u32 vcc, 0, v251\n"
"s_sub_u32 s90, s90, 1\n"
"s_cmp_eq_u32 s90, 0\n"
"s_cbranch_scc1 .Lret_cs\n"
"s_cbranch_vccnz .Lspin_cs\n"
"s_nop 4\n"
"s_branch .Lret_cs\n"
".Lslow_c13:\n"
"s_mov_b32 s90, 0x100000\n"
".Lspin_c13:\n"
"ds_read_b32 v252, v251\n"
"s_waitcnt lgkmcnt(0)\n"
"v_cmp_gt_u32 vcc, 4, v252\n"
"s_sub_u32 s90, s90, 1\n"
"s_cmp_eq_u32 s90, 0\n"
"s_cbranch_scc1 .Lret_c13\n"
"s_cbranch_vccnz .Lspin_c13\n"
"s_nop 4\n"
"s_branch .Lret_c13\n"
".Lslow_c14:\n"
"s_mov_b32 s90, 0x100000\n"
".Lspin_c14:\n"
"ds_read_b32 v250, v248\n"
"s_waitcnt lgkmcnt(0)\n"
"v_cmp_gt_u32 vcc, 52, v250\n"
"s_sub_u32 s90, s90, 1\n"
"s_cmp_eq_u32 s90, 0\n"
"s_cbranch_scc1 .Lret_c14\n"
"s_cbranch_vccnz .Lspin_c14\n"
"s_nop 4\n"
"s_branch .Lret_c14\n"
".Lslow_g15:\n"
"s_mov_b32 s90, 0x100000\n"
".Lspin_g15:\n"
"s_sleep 10\n"
"buffer_load_dwordx2 v[234:235], v236, s[52:55], 0 offen sc1\n"
"s_waitcnt vmcnt(0) lgkmcnt(0)\n"
"v_cmp_ne_u64 vcc, s[60:61], v[234:235]\n"
"s_sub_u32 s90, s90, 1\n"
"s_cmp_eq_u32 s90, 0\n"
"s_cbranch_scc1 .Lret_g15\n"
"s_cbranch_vccnz .Lspin_g15\n"
"s_nop 4\n"
"s_branch .Lret_g15\n"
".Lslow_c16:\n"
"s_mov_b32 s90, 0x100000\n"
".Lspin_c16:\n"
"ds_read_b32 v250, v248\n"
"s_waitcnt lgkmcnt(0)\n"
"v_cmp_gt_u32 vcc, 2, v250\n"
"s_sub_u32 s90, s90, 1\n"
"s_cmp_eq_u32 s90, 0\n"
"s_cbranch_scc1 .Lret_c16\n"
"s_cbranch_vccnz .Lspin_c16\n"
"s_nop 4\n"
"s_branch .Lret_c16\n"
".Lslow_c17:\n"
"s_mov_b32 s90, 0x100000\n"
".Lspin_c17:\n"
"ds_read_b32 v250, v248\n"
"s_waitcnt lgkmcnt(0)\n"
"v_cmp_gt_u32 vcc, 4, v250\n"
"s_sub_u32 s90, s90, 1\n"
"s_cmp_eq_u32 s90, 0\n"
"s_cbranch_scc1 .Lret_c17\n"
"s_cbranch_vccnz .Lspin_c17\n"
"s_nop 4\n"
"s_branch .Lret_c17\n"
".Lslow_g18:\n"
"s_mov_b32 s90, 0x100000\n"
".Lspin_g18:\n"
"s_sleep 12\n"
"s_setprio 3\n"
"buffer_load_dwordx2 v[216:217], v218, s[52:55], 0 offen sc1\n"
"s_setprio 0\n"
"s_waitcnt vmcnt(0) lgkmcnt(0)\n"
"v_cmp_ne_u64 vcc, s[60:61], v[216:217]\n"
"s_sub_u32 s90, s90, 1\n"
"s_cmp_eq_u32 s90, 0\n"
"s_cbranch_scc1 .Lret_g18\n"
"s_cbranch_vccnz .Lspin_g18\n"
"s_nop 4\n"
"s_branch .Lret_g18\n"
".Lslow_c19:\n"
"s_mov_b32 s90, 0x100000\n"
".Lspin_c19:\n"
"ds_read_b32 v250, v248\n"
"s_waitcnt lgkmcnt(0)\n"
"v_cmp_gt_u32 vcc, 6, v250\n"
"s_sub_u32 s90, s90, 1\n"
"s_cmp_eq_u32 s90, 0\n"
"s_cbranch_scc1 .Lret_c19\n"
"s_cbranch_vccnz .Lspin_c19\n"
"s_nop 4\n"
"s_branch .Lret_c19\n"
".Lslow_c20:\n"
"s_mov_b32 s90, 0x100000\n"
".Lspin_c20:\n"
"ds_read_b32 v250, v248\n"
"s_waitcnt lgkmcnt(0)\n"
"v_cmp_gt_u32 vcc, 8, v250\n"
"s_sub_u32 s90, s90, 1\n"
"s_cmp_eq_u32 s90, 0\n"
"s_cbranch_scc1 .Lret_c20\n"
"s_cbranch_vccnz .Lspin_c20\n"
"s_nop 4\n"
"s_branch .Lret_c20\n"
".Lslow_c21:\n"
"s_mov_b32 s90, 0x100000\n"
".Lspin_c21:\n"
"ds_read_b32 v250, v248\n"
"s_waitcnt lgkmcnt(0)\n"
"v_cmp_gt_u32 vcc, 10, v250\n"
"s_sub_u32 s90, s90, 1\n"
"s_cmp_eq_u32 s90, 0\n"
"s_cbranch_scc1 .Lret_c21\n"
"s_cbranch_vccnz .Lspin_c21\n"
"s_nop 4\n"
"s_branch .Lret_c21\n"
".Lslow_c22:\n"
"s_mov_b32 s90, 0x100000\n"
".Lspin_c22:\n"
"ds_read_b32 v250, v227\n"
"s_waitcnt lgkmcnt(0)\n"
"v_cmp_gt_u32 vcc, 4, v250\n"
"s_sub_u32 s90, s90, 1\n"
"s_cmp_eq_u32 s90, 0\n"
"s_cbranch_scc1 .Lret_c22\n"
"s_cbranch_vccnz .Lspin_c22\n"
"s_nop 4\n"
"s_branch .Lret_c22\n"
".Lslow_c23:\n"
"s_mov_b32 s90, 0x100000\n"
".Lspin_c23:\n"
"s_sleep 3\n"
"ds_read_b32 v250, v255\n"
"s_waitcnt lgkmcnt(0)\n"
"v_cmp_gt_u32 vcc, 1, v250\n"
"s_sub_u32 s90, s90, 1\n"
"s_cmp_eq_u32 s90, 0\n"
"s_cbranch_scc1 .Lret_c23\n"
"s_cbranch_vccnz .Lspin_c23\n"
"s_nop 4\n"
"s_branch .Lret_c23\n"
".Lslow_g24:\n"
"s_mov_b32 s90, 0x100000\n"
".Lspin_g24:\n"
"s_sleep 8\n"
"s_setprio 3\n"
"buffer_load_dwordx2 v[152:153], v154, s[52:55], 0 offen sc1\n"
"s_setprio 0\n"
"s_waitcnt vmcnt(0) lgkmcnt(0)\n"
"v_cmp_ne_u64 vcc, s[60:61], v[152:153]\n"
"s_sub_u32 s90, s90, 1\n"
"s_cmp_eq_u32 s90, 0\n"
"s_cbranch_scc1 .Lret_g24\n"
"s_cbranch_vccnz .Lspin_g24\n"
"s_nop 4\n"
"s_branch .Lret_g24\n"
".Lend:\n"
    ".Lhelp:\n"
    : "+s"(kp), "+s"(b), "+s"(w) : "s"(&GT[0]) : "v0","v1","v2","v3","v4","v5","v6","v7","v8","v9","v10","v11","v12","v13","v14","v15","v16","v17","v18","v19","v20","v21","v22","v23","v24","v25","v26","v27","v28","v29","v30","v31","v32","v33","v34","v35","v36","v37","v38","v39","v40","v41","v42","v43","v44","v45","v46","v47","v48","v49","v50","v51","v52","v53","v54","v55","v56","v57","v58","v59","v60","v61","v62","v63","v64","v65","v66","v67","v68","v69","v70","v71","v72","v73","v74","v75","v76","v77","v78","v79","v80","v81","v82","v83","v84","v85","v86","v87","v88","v89","v90","v91","v92","v93","v94","v95","v96","v97","v98","v99","v100","v101","v102","v103","v104","v105","v106","v107","v108","v109","v110","v111","v112","v113","v114","v115","v116","v117","v118","v119","v120","v121","v122","v123","v124","v125","v126","v127","v128","v129","v130","v131","v132","v133","v134","v135","v136","v137","v138","v139","v140","v141","v142","v143","v144","v145","v146","v147","v148","v149","v150","v151","v152","v153","v154","v155","v156","v157","v158","v159","v160","v161","v162","v163","v164","v165","v166","v167","v168","v169","v170","v171","v172","v173","v174","v175","v176","v177","v178","v179","v180","v181","v182","v183","v184","v185","v186","v187","v188","v189","v190","v191","v192","v193","v194","v195","v196","v197","v198","v199","v200","v201","v202","v203","v204","v205","v206","v207","v208","v209","v210","v211","v212","v213","v214","v215","v216","v217","v218","v219","v220","v221","v222","v223","v224","v225","v226","v227","v228","v229","v230","v231","v232","v233","v234","v235","v236","v237","v238","v239","v240","v241","v242","v243","v244","v245","v246","v247","v248","v249","v250","v251","v252","v253","v254","v255","s16","s17","s18","s19","s20","s21","s22","s23","s24","s25","s26","s27","s28","s29","s30","s31","s34","s35","s36","s37","s38","s39","s40","s41","s42","s43","s44","s45","s46","s47","s48","s49","s50","s51","s52","s53","s54","s55","s56","s57","s58","s59","s60","s61","s62","s63","s64","s65","s66","s67","s68","s69","s70","s71","s72","s73","s74","s75","s76","s77","s78","s79","s80","s81","s82","s83","s84","s85","s86","s87","s88","s89","s90","s91","s92","s93","s94","s95","s96","s97","s98","s99","vcc","scc","memory");
  int l; asm volatile("v_mbcnt_lo_u32_b32 %0, -1, 0\n v_mbcnt_hi_u32_b32 %0, -1, %0":"=v"(l));
  helper((const u16*)kp[0],(const u16*)kp[7],(const u16*)kp[8],(const u16*)kp[9],(char*)kp[11],sm,b,w,l);
}

extern "C" void kernel_launch(void* const* d_in,const int* in_sizes,int n_in,void* d_out,int out_size,void* d_ws,size_t ws_size,hipStream_t stream){
  fused_kernel<<<256,512,0,stream>>>((const u16*)d_in[0],(const u16*)d_in[1],(const u16*)d_in[2],(const u16*)d_in[3],(const u16*)d_in[4],(const u16*)d_in[5],(const u16*)d_in[6],
                                     (const u16*)d_in[8],(const u16*)d_in[9],(const u16*)d_in[10],(u16*)d_out,(char*)d_ws);
}
```
